```python
import torch

def _softmax(x, axis=-1):
    return torch.softmax(x, dim=axis)
def _gelu(x):
    return torch.nn.functional.gelu(x, approximate="tanh")
def _silu(x):
    return torch.nn.functional.silu(x)
def _sigmoid(x):
    return torch.sigmoid(x)
def _layernorm(x, axis=-1, eps=1e-5):
    m = x.mean(dim=axis, keepdim=True)
    v = x.var(dim=axis, keepdim=True, unbiased=False)
    return (x - m) / torch.sqrt(v + eps)
def _scatter_add(tgt, idx, src, axis=0):
    return tgt.clone().index_add_(axis, idx, src)
def _segment_sum(src, seg, n):
    out = torch.zeros((n,) + src.shape[1:], dtype=src.dtype)
    return out.index_add_(0, seg, src)
def _pad(x, pw):
    flat = tuple(v for pair in reversed(pw) for v in pair)
    return torch.nn.functional.pad(x, flat)

kernel_name = 'concat_heads__linear_kernel_hedgehog__ssm_conv_s5__prompt'

def setup_inputs():

    _w = (lambda a: (lambda t: t.to(torch.bfloat16) if t.is_floating_point() else t)(torch.as_tensor(a)))
    def _A_setup():
        B, S, D, H = 1, 2048, 1024, 32
        Dh = D // H
        return {
            "x": _w((lambda *s: torch.randn(*s, dtype=torch.bfloat16) * 0.4)(B, S, D)),
            "wqkv": _w((lambda *s: torch.randn(*s, dtype=torch.bfloat16) * 0.4)(D, 3 * D)),
            "w_phi": _w((lambda *s: torch.randn(*s, dtype=torch.bfloat16) * 0.4)(H, Dh, Dh) * (Dh ** -0.5)),
            "wo": _w((lambda *s: torch.randn(*s, dtype=torch.bfloat16) * 0.05)(D, D)),
        }
    _A_in = _A_setup()

    def _B_setup():
        B, S, D = 1, 2048, 1024
        P = D
        Lambda_re = -0.5 * (lambda _x: torch.exp(torch.as_tensor(_x)))((lambda *s: torch.randn(*s, dtype=torch.bfloat16) * 0.8)(P))
        Lambda_im = (lambda *s: torch.randn(*s, dtype=torch.bfloat16) * 0.8)(P) * 1.0
        B_re = (lambda *s: torch.randn(*s, dtype=torch.bfloat16) * 0.8)(P, D) * (D ** -0.5)
        B_im = (lambda *s: torch.randn(*s, dtype=torch.bfloat16) * 0.8)(P, D) * (D ** -0.5)
        C_re = (lambda *s: torch.randn(*s, dtype=torch.bfloat16) * 0.8)(D, P) * (P ** -0.5)
        C_im = (lambda *s: torch.randn(*s, dtype=torch.bfloat16) * 0.8)(D, P) * (P ** -0.5)
        log_dt = (lambda *s: torch.randn(*s, dtype=torch.bfloat16) * 0.8)(P) * 0.3 - 2.0
        Dskip = (lambda *s: torch.randn(*s, dtype=torch.bfloat16) * 0.8)(D)
        return {
            "x": _w((lambda *s: torch.randn(*s, dtype=torch.bfloat16) * 0.05)(B, S, D)),
            "Lambda_re": _w(Lambda_re),
            "Lambda_im": _w(Lambda_im),
            "B_re": _w(B_re),
            "B_im": _w(B_im),
            "C_re": _w(C_re),
            "C_im": _w(C_im),
            "log_dt": _w(log_dt),
            "Dskip": _w(Dskip),
        }
    _B_in = _B_setup()

    return {
        "A_x": _A_in["x"],
        "A_wqkv": _A_in["wqkv"],
        "A_w_phi": _A_in["w_phi"],
        "A_wo": _A_in["wo"],
        "B_x": _B_in["x"],
        "B_Lambda_re": _B_in["Lambda_re"],
        "B_Lambda_im": _B_in["Lambda_im"],
        "B_B_re": _B_in["B_re"],
        "B_B_im": _B_in["B_im"],
        "B_C_re": _B_in["C_re"],
        "B_C_im": _B_in["C_im"],
        "B_log_dt": _B_in["log_dt"],
        "B_Dskip": _B_in["Dskip"],
    }

def reference(A_x, A_wqkv, A_w_phi, A_wo, B_x, B_Lambda_re, B_Lambda_im, B_B_re, B_B_im, B_C_re, B_C_im, B_log_dt, B_Dskip):
    def _A_ref(x, wqkv, w_phi, wo):
        B, S, D = x.shape
        H = 32; Dh = D // H
        qkv = (lambda x, *d: x.permute(*d))((x @ wqkv).reshape(B, S, 3, H, Dh), 2, 0, 3, 1, 4)
        q, k, v = qkv[0], qkv[1], qkv[2]
        pq = torch.einsum("bhsd,hdf->bhsf", q, w_phi)
        pk = torch.einsum("bhsd,hdf->bhsf", k, w_phi)
        phi_q = torch.concatenate([_softmax(pq, axis=-1), _softmax(-pq, axis=-1)], axis=-1)
        phi_k = torch.concatenate([_softmax(pk, axis=-1), _softmax(-pk, axis=-1)], axis=-1)
        kv = torch.einsum("bhsf,bhsd->bhfd", phi_k, v)
        z  = torch.einsum("bhsf->bhf", phi_k)
        num = torch.einsum("bhqf,bhfd->bhqd", phi_q, kv)
        den = torch.einsum("bhqf,bhf->bhq", phi_q, z)[..., None] + 1e-6
        o = num / den
        return (lambda x, *d: x.permute(*d))(o, 0, 2, 1, 3).reshape(B, S, D) @ wo

    def _B_ref(x, Lambda_re, Lambda_im, B_re, B_im, C_re, C_im, log_dt, Dskip):
        Bsz, L, H = x.shape
        Lam = Lambda_re + 1j * Lambda_im
        Bmat = B_re + 1j * B_im
        Cmat = C_re + 1j * C_im
        dt = torch.exp(log_dt)
        Lam_bar = torch.exp(dt * Lam)
        B_bar = ((Lam_bar - 1.0) / Lam)[:, None] * Bmat
        Bu = torch.einsum("bld,pd->blp", x + 0j, B_bar)
        h = torch.zeros_like(Bu[:, 0, :])
        hs = []
        for t in range(L):
            h = Lam_bar[None, :] * h + Bu[:, t, :]
            hs.append(h)
        hseq = torch.stack(hs, 1)
        y = 2.0 * torch.einsum("blp,dp->bld", hseq, Cmat).real + x * Dskip[None, None, :]
        return y

    a = _A_ref(A_x, A_wqkv, A_w_phi, A_wo)
    b = _B_ref(B_x, B_Lambda_re, B_Lambda_im, B_B_re, B_B_im, B_C_re, B_C_im, B_log_dt, B_Dskip)
    return torch.concatenate([a, b], axis=-1)
```

```cpp
#include <hip/hip_runtime.h>
#include <stdint.h>

#define LAS __attribute__((address_space(3)))
#define GAS __attribute__((address_space(1)))
#define DI __device__ __forceinline__

typedef unsigned short u16;
typedef unsigned long long u64;
typedef __bf16 bf16x8 __attribute__((ext_vector_type(8)));
typedef __bf16 bf16x2 __attribute__((ext_vector_type(2)));
typedef short s16x4 __attribute__((ext_vector_type(4)));
typedef short s16x8 __attribute__((ext_vector_type(8)));
typedef float f32x2 __attribute__((ext_vector_type(2)));
typedef float f32x4 __attribute__((ext_vector_type(4)));
typedef float f32x16 __attribute__((ext_vector_type(16)));
typedef unsigned u32x2 __attribute__((ext_vector_type(2)));
typedef unsigned u32x4 __attribute__((ext_vector_type(4)));

#define MFMA32(a, b, c) __builtin_amdgcn_mfma_f32_32x32x16_bf16((a), (b), (c), 0, 0, 0)
#define RLX_AGENT __ATOMIC_RELAXED, __HIP_MEMORY_SCOPE_AGENT

constexpr size_t WS_O    = 0;
constexpr size_t WS_H    = 4u << 20;
constexpr size_t WS_KVP  = 12u << 20;
constexpr size_t KVP_BYTES = 2 * (2048 + 64) * 4;
constexpr size_t WS_AGG  = 15u << 20;
constexpr size_t WS_FLAG = 16u << 20;
constexpr int FL_EPOCH_A = 0, FL_EPOCH_B = 1, FL_KV = 16  , FL_O = 16 + 128  , FL_H = 16 + 256  ;
constexpr int LDS_BYTES = 160 * 1024;
constexpr unsigned SPIN_CAP = 1u << 20;

DI float bf2f(u16 v) { return __uint_as_float((unsigned)v << 16); }
DI unsigned pk2(float lo, float hi) { f32x2 x = {lo, hi}; return __builtin_bit_cast(unsigned, __builtin_convertvector(x, bf16x2)); }
template <int S> DI bf16x8 pack8(const f32x16& x) {
  u32x4 p = {pk2(x[8 * S], x[8 * S + 1]), pk2(x[8 * S + 2], x[8 * S + 3]), pk2(x[8 * S + 4], x[8 * S + 5]), pk2(x[8 * S + 6], x[8 * S + 7])};
  return __builtin_bit_cast(bf16x8, p);
}
template <int N> DI void wait_vm() { asm volatile("s_waitcnt vmcnt(%0)" ::"i"(N) : "memory"); }
DI void wait_lgkm0() { asm volatile("s_waitcnt lgkmcnt(0)" ::: "memory"); }
DI void bar() { __builtin_amdgcn_s_barrier(); }
DI void cfence() { asm volatile("" ::: "memory"); }
DI void sync_lds() { wait_lgkm0(); bar(); cfence(); }
DI void glds16(const void* g, LAS char* l) { __builtin_amdgcn_global_load_lds((const GAS unsigned*)g, (LAS unsigned*)l, 16, 0, 0); }
DI bf16x8 lds_b128(LAS char* p) { return *(LAS bf16x8*)p; }
DI bf16x8 lds_tr2(LAS char* p0, LAS char* p1) {
  s16x4 lo = __builtin_amdgcn_ds_read_tr16_b64_v4i16((LAS s16x4*)p0);
  s16x4 hi = __builtin_amdgcn_ds_read_tr16_b64_v4i16((LAS s16x4*)p1);
  return __builtin_bit_cast(bf16x8, __builtin_shufflevector(lo, hi, 0, 1, 2, 3, 4, 5, 6, 7));
}
DI float xhalf(float v) { return __shfl_xor(v, 32); }
DI GAS u64* flagp(char* ws, int i) { return (GAS u64*)(ws + WS_FLAG + (size_t)i * 128); }

template <int NT, int S, int P, int SB, class IssueF, class CompF>
DI void gemm_pipeline(IssueF issue, CompF comp) {
#pragma unroll
  for (int s = 0; s < S - 1; ++s) issue(s, s * SB);
  int rd = 0, wr = (S - 1) * SB;
#pragma unroll 1
  for (int t = 0; t < NT - (S - 1); ++t) {
    wait_vm<(S - 2) * P>(); wait_lgkm0(); bar(); cfence();
    issue(t + S - 1, wr);
    comp(rd);
    rd += SB; if (rd == S * SB) rd = 0;
    wr += SB; if (wr == S * SB) wr = 0;
  }
  if constexpr (S > 3) { wait_vm<2 * P>(); wait_lgkm0(); bar(); cfence(); comp(rd); rd += SB; if (rd == S * SB) rd = 0; }
  if constexpr (S > 2) { wait_vm<1 * P>(); wait_lgkm0(); bar(); cfence(); comp(rd); rd += SB; if (rd == S * SB) rd = 0; }
  wait_vm<0>(); wait_lgkm0(); bar(); cfence(); comp(rd);
}

DI void dual_softmax(const f32x16& x, f32x16& pp, f32x16& pm) {
  float mx = x[0], mn = x[0];
#pragma unroll
  for (int i = 1; i < 16; ++i) { mx = fmaxf(mx, x[i]); mn = fminf(mn, x[i]); }
  mx = fmaxf(mx, xhalf(mx)); mn = fminf(mn, xhalf(mn));
  const float L2E = 1.4426950408889634f;
  const float mxs = mx * L2E, mns = mn * L2E;
  float sp = 0.f, sm = 0.f;
#pragma unroll
  for (int i = 0; i < 16; ++i) {
    pp[i] = __builtin_amdgcn_exp2f(fmaf(x[i], L2E, -mxs)); sp += pp[i];
    pm[i] = __builtin_amdgcn_exp2f(fmaf(x[i], -L2E, mns)); sm += pm[i];
  }
  sp += xhalf(sp); sm += xhalf(sm);
  const float rp = __builtin_amdgcn_rcpf(sp), rm = __builtin_amdgcn_rcpf(sm);
#pragma unroll
  for (int i = 0; i < 16; ++i) { pp[i] *= rp; pm[i] *= rm; }
}

DI void poll_flags(char* ws, int base, int n, u64 tag, int lane) {
  GAS u64* p = flagp(ws, base + (lane < n ? lane : 0));
  for (unsigned spins = 0; spins < SPIN_CAP; ++spins) {
    u64 v = __hip_atomic_load(p, RLX_AGENT);
    if (__all(v == tag)) break;
    __builtin_amdgcn_s_sleep(1);
  }
}

template <bool IS_A>
DI void phase2(LAS char* sm, const u16* __restrict__ Aop, const u16* __restrict__ W0, const u16* __restrict__ W1,
               const u16* __restrict__ Bx, const u16* __restrict__ Dskip, float* __restrict__ out, int rt2, int ct2, int wave, int lane) {
  constexpr int NT = IS_A ? 16 : 32;
  constexpr int APITCH = IS_A ? 2048 : 4096;
  constexpr int S = 4, SB = 32768;
  const int h = lane >> 5, kh = wave >> 2, wm = (wave >> 1) & 1, wn = wave & 1;
  const int r6 = 8 * wave + (lane >> 3);
  const unsigned rowchunk = (unsigned)(((lane & 7) ^ ((r6 >> 1) & 7)) * 16);
  const unsigned voffA = (unsigned)r6 * APITCH + rowchunk;
  const unsigned voffB = IS_A ? ((unsigned)r6 * 2048 + (unsigned)(((lane & 7) ^ (((r6 >> 1) & 1) << 2)) * 16))
                              : ((unsigned)r6 * 2048 + rowchunk);
  const char* gA = (const char*)Aop + (size_t)rt2 * 128 * APITCH;
  const char* gB0 = IS_A ? (const char*)W0 + (size_t)ct2 * 256 : (const char*)W0 + (size_t)ct2 * 128 * 2048;
  const char* gB1 = IS_A ? gB0 : (const char*)W1 + (size_t)ct2 * 128 * 2048;
  LAS char* dst0 = sm + wave * 1024;
  auto issue = [&](int kt, int buf) {
    const char* a = gA + (size_t)kt * 128;
    glds16(a + voffA, dst0 + buf);
    glds16(a + (size_t)64 * APITCH + voffA, dst0 + buf + 8192);
    if constexpr (IS_A) {
      const char* bb = gB0 + (size_t)kt * 64 * 2048;
      glds16(bb + voffB, dst0 + buf + 16384);
      glds16(bb + 128 + voffB, dst0 + buf + 24576);
    } else {
      const char* bb = (kt < 16 ? gB0 : gB1) + (size_t)(kt & 15) * 128;
      glds16(bb + voffB, dst0 + buf + 16384);
      glds16(bb + (size_t)64 * 2048 + voffB, dst0 + buf + 24576);
    }
  };
  const int xs = (lane >> 1) & 7;
  int rofs[2];
#pragma unroll
  for (int s = 0; s < 2; ++s) rofs[s] = (lane & 31) * 128 + 16 * ((2 * (2 * kh + s) + h) ^ xs);
  const int q = (lane & 15) >> 2, p4 = lane & 3, blk = (lane >> 4) & 1;
  const int tofs = (32 * kh + 16 * h + q) * 128 + 32 * blk + 8 * p4;
  f32x16 acc[2][2];
#pragma unroll
  for (int i = 0; i < 2; ++i)
#pragma unroll
    for (int j = 0; j < 2; ++j)
#pragma unroll
      for (int e = 0; e < 16; ++e) acc[i][j][e] = 0.f;
  auto comp = [&](int buf) {
    LAS char* sb = sm + buf;
#pragma unroll
    for (int s = 0; s < 2; ++s) {
      bf16x8 a0 = lds_b128(sb + rofs[s] + (64 * wm) * 128), a1 = lds_b128(sb + rofs[s] + (64 * wm + 32) * 128);
      bf16x8 b0, b1;
      if constexpr (IS_A) {
        LAS char* tb = sb + 16384 + wn * 8192 + tofs + s * 512;
        const int sw = 64 * ((q >> 1) & 1);
        b0 = lds_tr2(tb + (0 ^ sw), tb + 1024 + (0 ^ sw));
        b1 = lds_tr2(tb + (64 ^ sw), tb + 1024 + (64 ^ sw));
      } else {
        b0 = lds_b128(sb + 16384 + rofs[s] + (64 * wn) * 128); b1 = lds_b128(sb + 16384 + rofs[s] + (64 * wn + 32) * 128);
      }
      acc[0][0] = MFMA32(a0, b0, acc[0][0]); acc[0][1] = MFMA32(a0, b1, acc[0][1]);
      acc[1][0] = MFMA32(a1, b0, acc[1][0]); acc[1][1] = MFMA32(a1, b1, acc[1][1]);
    }
  };
  gemm_pipeline<NT, S, 4, SB>(issue, comp);
  sync_lds();
  {
    LAS char* mine = sm + wave * 8192 + lane * 16;
#pragma unroll
    for (int j = 0; j < 2; ++j)
#pragma unroll
      for (int g = 0; g < 4; ++g) {
        f32x4 v;
#pragma unroll
        for (int e = 0; e < 4; ++e) v[e] = kh ? acc[0][j][4 * g + e] : acc[1][j][4 * g + e];
        *(LAS f32x4*)(mine + (j * 4 + g) * 1024) = v;
      }
  }
  const int row0 = rt2 * 128 + 64 * wm + 32 * kh + 4 * h;
  const int col0 = ct2 * 128 + 64 * wn + (lane & 31);
  float xd[2][16];
  if constexpr (!IS_A) {
#pragma unroll
    for (int j = 0; j < 2; ++j) {
      const float dsk = bf2f(Dskip[col0 + 32 * j]);
#pragma unroll
      for (int i = 0; i < 16; ++i) xd[j][i] = bf2f(Bx[(size_t)(row0 + (i & 3) + 8 * (i >> 2)) * 1024 + col0 + 32 * j]) * dsk;
    }
  }
  sync_lds();
  {
    LAS char* theirs = sm + (wave ^ 4) * 8192 + lane * 16;
    float* op = out + (size_t)row0 * 2048 + (IS_A ? 0 : 1024) + col0;
#pragma unroll
    for (int j = 0; j < 2; ++j)
#pragma unroll
      for (int g = 0; g < 4; ++g) {
        f32x4 v = *(LAS f32x4*)(theirs + (j * 4 + g) * 1024);
#pragma unroll
        for (int e = 0; e < 4; ++e) {
          float r = v[e] + (kh ? acc[1][j][4 * g + e] : acc[0][j][4 * g + e]);
          if constexpr (!IS_A) r += xd[j][4 * g + e];
          op[(size_t)(e + 8 * g) * 2048 + 32 * j] = r;
        }
      }
  }
}

__global__ __launch_bounds__(512, 2) void fused_kernel(
    const u16* __restrict__ Ax, const u16* __restrict__ Wqkv, const u16* __restrict__ Wphi, const u16* __restrict__ Wo,
    const u16* __restrict__ Bx, const u16* __restrict__ Lre, const u16* __restrict__ Lim, const u16* __restrict__ Bre,
    const u16* __restrict__ Bim, const u16* __restrict__ Cre, const u16* __restrict__ Cim, const u16* __restrict__ Logdt,
    const u16* __restrict__ Dskip, float* __restrict__ out, char* ws) {
  __shared__ __attribute__((aligned(1024))) char smem[LDS_BYTES];
  LAS char* sm = (LAS char*)smem;
  const int tid = threadIdx.x, lane = tid & 63, wave = __builtin_amdgcn_readfirstlane(tid >> 6);
  const int b = blockIdx.x, xcd = b & 7, idx = b >> 3, x4 = xcd & 3;
  const bool isA = xcd < 4;
  const int rt = 4 * (x4 & 1) + (idx & 3), cg = 8 * (x4 >> 1) + (idx >> 2);
  const int rt2 = 4 * x4 + (idx & 3), ct2 = idx >> 2;
  const int h = lane >> 5, c32 = lane & 31, wm = wave >> 1, wn = wave & 1;
  const int r6 = 8 * wave + (lane >> 3);
  const unsigned rowchunk = (unsigned)(((lane & 7) ^ ((r6 >> 1) & 7)) * 16);
  const int xs = (lane >> 1) & 7;
  const int q = (lane & 15) >> 2, p4 = lane & 3, blk = (lane >> 4) & 1;
  const u64 tag = __hip_atomic_load(flagp(ws, isA ? FL_EPOCH_A : FL_EPOCH_B), RLX_AGENT) + 1ull;
  int rofs[4];
#pragma unroll
  for (int s = 0; s < 4; ++s) rofs[s] = c32 * 128 + 16 * ((2 * s + h) ^ xs);
  LAS char* dst0 = sm + wave * 1024;

  if (isA) {
    const int hd = 2 * cg + wn;
    u16 wraw[16];
#pragma unroll
    for (int i = 0; i < 16; ++i) wraw[i] = Wphi[(size_t)hd * 1024 + ((i & 3) + 8 * (i >> 2) + 4 * h) * 32 + c32];
    constexpr int SB = 57344;
    const unsigned voffA = (unsigned)r6 * 2048 + rowchunk;
    const unsigned voffB = (unsigned)r6 * 6144 + (unsigned)(((lane & 7) ^ (((r6 >> 1) & 1) << 2)) * 16);
    const char* gA = (const char*)Ax + (size_t)rt * 256 * 2048;
    const char* gB = (const char*)Wqkv + (size_t)cg * 128;
    auto issue = [&](int kt, int buf) {
      const char* a = gA + (size_t)kt * 128;
#pragma unroll
      for (int i = 0; i < 4; ++i) glds16(a + (size_t)i * 64 * 2048 + voffA, dst0 + buf + i * 8192);
      const char* bb = gB + (size_t)kt * 64 * 6144;
#pragma unroll
      for (int j = 0; j < 3; ++j) glds16(bb + j * 2048 + voffB, dst0 + buf + 32768 + j * 8192);
    };
    const int tofs = 32768 + (8 * h + q) * 128 + 64 * (wn ^ ((q >> 1) & 1)) + 32 * blk + 8 * p4;
    f32x16 Qt[2], Kt[2], V[2];
#pragma unroll
    for (int m = 0; m < 2; ++m)
#pragma unroll
      for (int e = 0; e < 16; ++e) { Qt[m][e] = 0.f; Kt[m][e] = 0.f; V[m][e] = 0.f; }
    auto comp = [&](int buf) {
      LAS char* sb = sm + buf;
#pragma unroll
      for (int s = 0; s < 4; ++s) {
        bf16x8 x0 = lds_b128(sb + rofs[s] + (64 * wm) * 128), x1 = lds_b128(sb + rofs[s] + (64 * wm + 32) * 128);
        LAS char* tb = sb + tofs + s * 2048;
        bf16x8 wq = lds_tr2(tb, tb + 512), wk = lds_tr2(tb + 8192, tb + 8192 + 512), wv = lds_tr2(tb + 16384, tb + 16384 + 512);
        Qt[0] = MFMA32(wq, x0, Qt[0]); Qt[1] = MFMA32(wq, x1, Qt[1]);
        Kt[0] = MFMA32(wk, x0, Kt[0]); Kt[1] = MFMA32(wk, x1, Kt[1]);
        V[0] = MFMA32(x0, wv, V[0]);   V[1] = MFMA32(x1, wv, V[1]);
      }
    };
    gemm_pipeline<16, 2, 7, SB>(issue, comp);
    sync_lds();

    bf16x8 wf0, wf1;
    {
      s16x8 t0, t1;
#pragma unroll
      for (int j = 0; j < 8; ++j) { t0[j] = (short)wraw[j]; t1[j] = (short)wraw[8 + j]; }
      wf0 = __builtin_bit_cast(bf16x8, t0); wf1 = __builtin_bit_cast(bf16x8, t1);
    }
    f32x16 zero16;
#pragma unroll
    for (int e = 0; e < 16; ++e) zero16[e] = 0.f;
    constexpr int PH_PITCH = 136, PH_BYTES = 64 * PH_PITCH;
    LAS char* ph = sm + wave * PH_BYTES;
#pragma unroll
    for (int m = 0; m < 2; ++m) {
      f32x16 pk = MFMA32(wf0, pack8<0>(Kt[m]), zero16);
      pk = MFMA32(wf1, pack8<1>(Kt[m]), pk);
      f32x16 pp, pm;
      dual_softmax(pk, pp, pm);
      LAS char* rowp = ph + (32 * m + c32) * PH_PITCH + 8 * h;
#pragma unroll
      for (int g = 0; g < 4; ++g) {
        u32x2 a = {pk2(pp[4 * g], pp[4 * g + 1]), pk2(pp[4 * g + 2], pp[4 * g + 3])};
        u32x2 c = {pk2(pm[4 * g], pm[4 * g + 1]), pk2(pm[4 * g + 2], pm[4 * g + 3])};
        *(LAS u32x2*)(rowp + 16 * g) = a;
        *(LAS u32x2*)(rowp + 64 + 16 * g) = c;
      }
    }
    wait_lgkm0();
    f32x16 KV[2], Z[2];
    KV[0] = zero16; KV[1] = zero16; Z[0] = zero16; Z[1] = zero16;
    {
      s16x8 o8;
#pragma unroll
      for (int j = 0; j < 8; ++j) o8[j] = (short)0x3f80;
      const bf16x8 ones = __builtin_bit_cast(bf16x8, o8);
      LAS char* tp = ph + (4 * h + q) * PH_PITCH + 32 * blk + 8 * p4;
#pragma unroll
      for (int m = 0; m < 2; ++m) {
#pragma unroll
        for (int s = 0; s < 2; ++s) {
          const bf16x8 vb = s ? pack8<1>(V[m]) : pack8<0>(V[m]);
          LAS char* t0 = tp + (32 * m + 16 * s) * PH_PITCH;
#pragma unroll
          for (int fs = 0; fs < 2; ++fs) {
            const bf16x8 af = lds_tr2(t0 + 64 * fs, t0 + 8 * PH_PITCH + 64 * fs);
            KV[fs] = MFMA32(af, vb, KV[fs]);
            Z[fs] = MFMA32(af, ones, Z[fs]);
          }
        }
      }
    }
    constexpr int RED_OFF = 8 * PH_BYTES;
    constexpr int RED_W = 8192 + 256;
    {
      LAS char* rw = sm + RED_OFF + wave * RED_W;
#pragma unroll
      for (int fs = 0; fs < 2; ++fs)
#pragma unroll
        for (int g = 0; g < 4; ++g) {
          f32x4 v = {KV[fs][4 * g], KV[fs][4 * g + 1], KV[fs][4 * g + 2], KV[fs][4 * g + 3]};
          *(LAS f32x4*)(rw + (fs * 4 + g) * 1024 + lane * 16) = v;
          if (c32 == 0) {
            f32x4 z = {Z[fs][4 * g], Z[fs][4 * g + 1], Z[fs][4 * g + 2], Z[fs][4 * g + 3]};
            *(LAS f32x4*)(rw + 8192 + ((fs * 2 + h) * 4 + g) * 16) = z;
          }
        }
    }
    sync_lds();
    const auto kvrs = __builtin_amdgcn_make_buffer_rsrc(ws + WS_KVP + (size_t)cg * 8 * KVP_BYTES, 0, (int)(8 * KVP_BYTES), 0x00020000);
    {
#pragma unroll
      for (int hh = 0; hh < 2; ++hh) {
        f32x4 s = {0.f, 0.f, 0.f, 0.f};
#pragma unroll
        for (int w = 0; w < 4; ++w) s += *(LAS f32x4*)(sm + RED_OFF + (2 * w + hh) * RED_W + tid * 16);
        __builtin_amdgcn_raw_buffer_store_b128(__builtin_bit_cast(u32x4, s), kvrs, rt * (int)KVP_BYTES + hh * 8448 + tid * 16, 0, 16);
      }
      if (tid < 32) {
        const int hh = tid >> 4;
        f32x4 s = {0.f, 0.f, 0.f, 0.f};
#pragma unroll
        for (int w = 0; w < 4; ++w) s += *(LAS f32x4*)(sm + RED_OFF + (2 * w + hh) * RED_W + 8192 + (tid & 15) * 16);
        __builtin_amdgcn_raw_buffer_store_b128(__builtin_bit_cast(u32x4, s), kvrs, rt * (int)KVP_BYTES + hh * 8448 + 8192 + (tid & 15) * 16, 0, 16);
      }
    }
    wait_vm<0>();
    sync_lds();
    if (tid == 0) __hip_atomic_store(flagp(ws, FL_KV + cg * 8 + rt), tag, RLX_AGENT);
    bf16x8 fq[2][4];
#pragma unroll
    for (int m = 0; m < 2; ++m) {
      f32x16 pq = MFMA32(wf0, pack8<0>(Qt[m]), zero16);
      pq = MFMA32(wf1, pack8<1>(Qt[m]), pq);
      f32x16 pp, pm;
      dual_softmax(pq, pp, pm);
      fq[m][0] = pack8<0>(pp); fq[m][1] = pack8<1>(pp); fq[m][2] = pack8<0>(pm); fq[m][3] = pack8<1>(pm);
    }
    if (wave == 0) poll_flags(ws, FL_KV + cg * 8, 8, tag, lane);
    cfence(); bar(); cfence();
    constexpr int KVF_OFF = 0;
    {
#pragma unroll
      for (int hh = 0; hh < 2; ++hh) {
        f32x4 v[8];
#pragma unroll
        for (int r = 0; r < 8; ++r)
          v[r] = __builtin_bit_cast(f32x4, __builtin_amdgcn_raw_buffer_load_b128(kvrs, hh * 8448 + tid * 16, r * (int)KVP_BYTES, 16));
        f32x4 s = ((v[0] + v[1]) + (v[2] + v[3])) + ((v[4] + v[5]) + (v[6] + v[7]));
        u32x2 w = {pk2(s[0], s[1]), pk2(s[2], s[3])};
        *(LAS u32x2*)(sm + KVF_OFF + hh * 4096 + (wave >> 1) * 1024 + lane * 16 + (wave & 1) * 8) = w;
      }
      if (tid < 32) {
        const int hh = tid >> 4, z = tid & 15;
        f32x4 v[8];
#pragma unroll
        for (int r = 0; r < 8; ++r)
          v[r] = __builtin_bit_cast(f32x4, __builtin_amdgcn_raw_buffer_load_b128(kvrs, hh * 8448 + 8192 + z * 16, r * (int)KVP_BYTES, 16));
        f32x4 s = ((v[0] + v[1]) + (v[2] + v[3])) + ((v[4] + v[5]) + (v[6] + v[7]));
        u32x2 w = {pk2(s[0], s[1]), pk2(s[2], s[3])};
        const int fs = z >> 3, zh = (z >> 2) & 1, g = z & 3;
        *(LAS u32x2*)(sm + KVF_OFF + 8192 + hh * 128 + ((fs * 2 + (g >> 1)) * 2 + zh) * 16 + (g & 1) * 8) = w;
      }
    }
    sync_lds();
    {
      const auto ors = __builtin_amdgcn_make_buffer_rsrc(ws + WS_O, 0, 2048 * 2048, 0x00020000);
      bf16x8 kf[4], zf[4];
#pragma unroll
      for (int ks = 0; ks < 4; ++ks) {
        kf[ks] = lds_b128(sm + KVF_OFF + wn * 4096 + ks * 1024 + lane * 16);
        zf[ks] = lds_b128(sm + KVF_OFF + 8192 + wn * 128 + (ks * 2 + h) * 16);
      }
#pragma unroll
      for (int m = 0; m < 2; ++m) {
        f32x16 num = zero16, den = zero16;
#pragma unroll
        for (int ks = 0; ks < 4; ++ks) { num = MFMA32(kf[ks], fq[m][ks], num); den = MFMA32(zf[ks], fq[m][ks], den); }
        const float rinv = 1.0f / (den[0] + 1e-6f);
#pragma unroll
        for (int e = 0; e < 16; ++e) num[e] *= rinv;
        const int row = rt * 256 + 64 * wm + 32 * m + c32;
        const int off = row * 2048 + hd * 64 + h * 32;
        __builtin_amdgcn_raw_buffer_store_b128(__builtin_bit_cast(u32x4, pack8<0>(num)), ors, off, 0, 16);
        __builtin_amdgcn_raw_buffer_store_b128(__builtin_bit_cast(u32x4, pack8<1>(num)), ors, off + 16, 0, 16);
      }
    }
    wait_vm<0>();
    sync_lds();
    if (tid == 0) __hip_atomic_store(flagp(ws, FL_O + rt * 16 + cg), tag, RLX_AGENT);
    if (wave == 0) {
      poll_flags(ws, FL_O + (rt2 >> 1) * 16, 16, tag, lane);
      __builtin_amdgcn_fence(__ATOMIC_ACQUIRE, "agent");
      wait_vm<0>();
      if (b == 0 && lane == 0) __hip_atomic_store(flagp(ws, FL_EPOCH_A), tag, RLX_AGENT);
    }
    cfence(); bar(); cfence();
    phase2<true>(sm, (const u16*)(ws + WS_O), Wo, Wo, Bx, Dskip, out, rt2, ct2, wave, lane);
  } else {
    const int ch = 64 * cg + 32 * wn + c32;
    const u16 lre_raw = Lre[ch], lim_raw = Lim[ch], ldt_raw = Logdt[ch];
    constexpr int SB = 49152, S = 3;
    const int rho = r6 & 31, tloc = 32 * ((rho >> 2) & 1) + 16 * (r6 >> 5) + (rho & 3) + 4 * (rho >> 3);
    const unsigned voffA = (unsigned)tloc * 2048 + rowchunk;
    const unsigned voffB = (unsigned)r6 * 2048 + rowchunk;
    const char* gA = (const char*)Bx + (size_t)rt * 256 * 2048;
    const char* gBr = (const char*)Bre + (size_t)cg * 64 * 2048;
    const char* gBi = (const char*)Bim + (size_t)cg * 64 * 2048;
    auto issue = [&](int kt, int buf) {
      const char* a = gA + (size_t)kt * 128;
#pragma unroll
      for (int i = 0; i < 4; ++i) glds16(a + (size_t)i * 64 * 2048 + voffA, dst0 + buf + i * 8192);
      glds16(gBr + (size_t)kt * 128 + voffB, dst0 + buf + 32768);
      glds16(gBi + (size_t)kt * 128 + voffB, dst0 + buf + 40960);
    };
    f32x16 Ur[2], Ui[2];
#pragma unroll
    for (int m = 0; m < 2; ++m)
#pragma unroll
      for (int e = 0; e < 16; ++e) { Ur[m][e] = 0.f; Ui[m][e] = 0.f; }
    auto comp = [&](int buf) {
      LAS char* sb = sm + buf;
#pragma unroll
      for (int s = 0; s < 4; ++s) {
        bf16x8 x0 = lds_b128(sb + rofs[s] + (64 * wm) * 128), x1 = lds_b128(sb + rofs[s] + (64 * wm + 32) * 128);
        bf16x8 br = lds_b128(sb + rofs[s] + 32768 + (32 * wn) * 128), bi = lds_b128(sb + rofs[s] + 40960 + (32 * wn) * 128);
        Ur[0] = MFMA32(x0, br, Ur[0]); Ui[0] = MFMA32(x0, bi, Ui[0]);
        Ur[1] = MFMA32(x1, br, Ur[1]); Ui[1] = MFMA32(x1, bi, Ui[1]);
      }
    };
    gemm_pipeline<16, S, 6, SB>(issue, comp);

    const float lamr = bf2f(lre_raw), lami = bf2f(lim_raw);
    const float dt = expf(bf2f(ldt_raw));
    float lr, li, c2r, c2i, l32r, l32i, l256r, l256i;
    {
      const float zr = dt * lamr, zi = dt * lami;
      float sn, cs;
      sincosf(zi, &sn, &cs);
      const float mag = expf(zr);
      lr = mag * cs; li = mag * sn;
      const float nr = lr - 1.0f, ni = li, dd = 2.0f / (lamr * lamr + lami * lami);
      c2r = (nr * lamr + ni * lami) * dd; c2i = (ni * lamr - nr * lami) * dd;
      float pr = lr, pi = li;
#pragma unroll
      for (int i = 0; i < 5; ++i) { const float t = pr * pr - pi * pi; pi = 2.0f * pr * pi; pr = t; }
      l32r = pr; l32i = pi;
#pragma unroll
      for (int i = 0; i < 3; ++i) { const float t = pr * pr - pi * pi; pi = 2.0f * pr * pi; pr = t; }
      l256r = pr; l256i = pi;
    }
    float er = 0.f, ei = 0.f;
#pragma unroll
    for (int m = 0; m < 2; ++m)
#pragma unroll
      for (int i = 0; i < 16; ++i) {
        const float nr = fmaf(lr, er, fmaf(-li, ei, Ur[m][i])), ni = fmaf(lr, ei, fmaf(li, er, Ui[m][i]));
        er = nr; ei = ni;
      }
    constexpr int TE_OFF = S * SB;
    constexpr int TG_OFF = TE_OFF + 4096;
    const int seg = 2 * wm + h, chl = 32 * wn + c32;
    *(LAS f32x2*)(sm + TE_OFF + (seg * 64 + chl) * 8) = (f32x2){er, ei};
    sync_lds();
    f32x2 E[8];
#pragma unroll
    for (int s = 0; s < 8; ++s) E[s] = *(LAS f32x2*)(sm + TE_OFF + (s * 64 + chl) * 8);
    const auto agrs = __builtin_amdgcn_make_buffer_rsrc(ws + WS_AGG + (size_t)cg * 8192, 0, 8192, 0x00020000);
    if (wm == 0) {
      float tr = 0.f, ti = 0.f;
#pragma unroll
      for (int s = 0; s < 8; ++s) { const float nr = fmaf(l32r, tr, fmaf(-l32i, ti, E[s][0])), ni = fmaf(l32r, ti, fmaf(l32i, tr, E[s][1])); tr = nr; ti = ni; }
      if (h == 0) {
        u32x4 gr = {__float_as_uint(tr), __float_as_uint(ti), (unsigned)tag, (unsigned)(tag >> 32)};
        __builtin_amdgcn_raw_buffer_store_b128(gr, agrs, (rt * 64 + chl) * 16, 0, 16);
      }
      for (unsigned spins = 0; spins < SPIN_CAP; ++spins) {
        bool ok = true;
#pragma unroll
        for (int k = 0; k < 4; ++k) {
          const int r = 2 * k + h;
          if (r < rt) {
            u32x4 gr = __builtin_amdgcn_raw_buffer_load_b128(agrs, (r * 64 + chl) * 16, 0, 16);
            ok &= (gr[2] == (unsigned)tag) & (gr[3] == (unsigned)(tag >> 32));
            *(LAS f32x2*)(sm + TG_OFF + (r * 64 + chl) * 8) = (f32x2){__uint_as_float(gr[0]), __uint_as_float(gr[1])};
          }
        }
        if (__all(ok)) break;
        __builtin_amdgcn_s_sleep(1);
      }
    }
    sync_lds();
    er = 0.f; ei = 0.f;
    for (int r = 0; r < rt; ++r) {
      const f32x2 g = *(LAS f32x2*)(sm + TG_OFF + (r * 64 + chl) * 8);
      const float nr = fmaf(l256r, er, fmaf(-l256i, ei, g[0])), ni = fmaf(l256r, ei, fmaf(l256i, er, g[1]));
      er = nr; ei = ni;
    }
#pragma unroll
    for (int s = 0; s < 7; ++s) {
      const float nr = fmaf(l32r, er, fmaf(-l32i, ei, E[s][0])), ni = fmaf(l32r, ei, fmaf(l32i, er, E[s][1]));
      if (s < seg) { er = nr; ei = ni; }
    }
    {
      const auto hrs = __builtin_amdgcn_make_buffer_rsrc(ws + WS_H, 0, 2048 * 4096, 0x00020000);
      const int odd = lane & 1;
      const int voff = (rt * 256 + 64 * wm + 32 * h) * 4096 + (odd ? 2048 + 2 * (ch - 1) : 2 * ch);
      const unsigned sel = odd ? 0x03020706u : 0x05040100u;
#pragma unroll
      for (int m = 0; m < 2; ++m)
#pragma unroll
        for (int i = 0; i < 16; ++i) {
          const float nr = fmaf(lr, er, fmaf(-li, ei, Ur[m][i])), ni = fmaf(lr, ei, fmaf(li, er, Ui[m][i]));
          er = nr; ei = ni;
          const float hr = c2r * er - c2i * ei, hi = -(c2r * ei + c2i * er);
          const unsigned own = pk2(hr, hi);
          const unsigned nb = (unsigned)__builtin_amdgcn_update_dpp(0, (int)own, 0xB1, 0xF, 0xF, false);
          const unsigned w = __builtin_amdgcn_perm(nb, own, sel);
          __builtin_amdgcn_raw_buffer_store_b32(w, hrs, voff, (16 * m + i) * 4096, 16);
        }
    }
    wait_vm<0>();
    sync_lds();
    if (tid == 0) __hip_atomic_store(flagp(ws, FL_H + rt * 16 + cg), tag, RLX_AGENT);
    if (wave == 0) {
      poll_flags(ws, FL_H + (rt2 >> 1) * 16, 16, tag, lane);
      __builtin_amdgcn_fence(__ATOMIC_ACQUIRE, "agent");
      wait_vm<0>();
      if (rt2 == 15 && ct2 == 0 && lane == 0) __hip_atomic_store(flagp(ws, FL_EPOCH_B), tag, RLX_AGENT);
    }
    cfence(); bar(); cfence();
    phase2<false>(sm, (const u16*)(ws + WS_H), Cre, Cim, Bx, Dskip, out, rt2, ct2, wave, lane);
  }
}

extern "C" void kernel_launch(void* const* d_in, const int* in_sizes, int n_in, void* d_out, int out_size, void* d_ws,
                              size_t ws_size, hipStream_t stream) {
  fused_kernel<<<256, 512, 0, stream>>>((const u16*)d_in[0], (const u16*)d_in[1], (const u16*)d_in[2], (const u16*)d_in[3],
                                        (const u16*)d_in[4], (const u16*)d_in[5], (const u16*)d_in[6], (const u16*)d_in[7],
                                        (const u16*)d_in[8], (const u16*)d_in[9], (const u16*)d_in[10], (const u16*)d_in[11],
                                        (const u16*)d_in[12], (float*)d_out, (char*)d_ws);
}
```

```cpp
#include <hip/hip_runtime.h>
#include <stdint.h>

#define LAS __attribute__((address_space(3)))
#define GAS __attribute__((address_space(1)))
#define DI __device__ __forceinline__

typedef unsigned short u16;
typedef unsigned long long u64;
typedef __bf16 bf16x8 __attribute__((ext_vector_type(8)));
typedef __bf16 bf16x2 __attribute__((ext_vector_type(2)));
typedef short s16x4 __attribute__((ext_vector_type(4)));
typedef short s16x8 __attribute__((ext_vector_type(8)));
typedef float f32x2 __attribute__((ext_vector_type(2)));
typedef float f32x4 __attribute__((ext_vector_type(4)));
typedef float f32x16 __attribute__((ext_vector_type(16)));
typedef unsigned u32x2 __attribute__((ext_vector_type(2)));
typedef unsigned u32x4 __attribute__((ext_vector_type(4)));

#define MFMA32(a, b, c) __builtin_amdgcn_mfma_f32_32x32x16_bf16((a), (b), (c), 0, 0, 0)
#define RLX_AGENT __ATOMIC_RELAXED, __HIP_MEMORY_SCOPE_AGENT

constexpr size_t WS_O    = 0;
constexpr size_t WS_H    = 4u << 20;
constexpr size_t WS_KVP  = 12u << 20;
constexpr int    KVP_BYTES = 2 * 8448;
constexpr size_t WS_AGG  = 15u << 20;
constexpr size_t WS_FLAG = 16u << 20;
constexpr int FL_EPOCH_A = 0, FL_EPOCH_B = 256;
constexpr int FL_KV = 4096;
constexpr int FL_O = 32768;
constexpr int FL_H = 65536;
constexpr int LDS_BYTES = 160 * 1024;
constexpr unsigned SPIN_CAP = 1u << 20;
#define PFD 6

#define STAMP(i) do {} while (0)
#define STAMP_FLUSH() do {} while (0)

DI float bf2f(u16 v) { return __uint_as_float((unsigned)v << 16); }
DI float bflo(unsigned v) { return __uint_as_float(v << 16); }
DI float bfhi(unsigned v) { return __uint_as_float(v & 0xffff0000u); }
DI unsigned pk2(float lo, float hi) { f32x2 x = {lo, hi}; return __builtin_bit_cast(unsigned, __builtin_convertvector(x, bf16x2)); }
template <int S> DI bf16x8 pack8(const f32x16& x) {
  u32x4 p = {pk2(x[8 * S], x[8 * S + 1]), pk2(x[8 * S + 2], x[8 * S + 3]), pk2(x[8 * S + 4], x[8 * S + 5]), pk2(x[8 * S + 6], x[8 * S + 7])};
  return __builtin_bit_cast(bf16x8, p);
}
template <int N> DI void wait_vm() { asm volatile("s_waitcnt vmcnt(%0)" ::"i"(N) : "memory"); }
DI void wait_lgkm0() { asm volatile("s_waitcnt lgkmcnt(0)" ::: "memory"); }
DI void bar() { __builtin_amdgcn_s_barrier(); }
DI void cfence() { asm volatile("" ::: "memory"); }
DI void sync_lds() { wait_lgkm0(); bar(); cfence(); }
DI bf16x8 lds_b128(LAS char* p) { return *(LAS bf16x8*)p; }
DI s16x4 lds_tr(LAS char* p) { return __builtin_amdgcn_ds_read_tr16_b64_v4i16((LAS s16x4*)p); }
DI bf16x8 cat8(s16x4 lo, s16x4 hi) { return __builtin_bit_cast(bf16x8, __builtin_shufflevector(lo, hi, 0, 1, 2, 3, 4, 5, 6, 7)); }
DI bf16x8 zero8() { s16x8 z = {0, 0, 0, 0, 0, 0, 0, 0}; return __builtin_bit_cast(bf16x8, z); }
DI GAS u64* flagp(char* ws, int off) { return (GAS u64*)(ws + WS_FLAG + off); }
DI float hmax(float v) { auto r = __builtin_amdgcn_permlane32_swap(__float_as_uint(v), __float_as_uint(v), false, false); return fmaxf(__uint_as_float(r[0]), __uint_as_float(r[1])); }
DI float hmin(float v) { auto r = __builtin_amdgcn_permlane32_swap(__float_as_uint(v), __float_as_uint(v), false, false); return fminf(__uint_as_float(r[0]), __uint_as_float(r[1])); }
DI float hsum(float v) { auto r = __builtin_amdgcn_permlane32_swap(__float_as_uint(v), __float_as_uint(v), false, false); return __uint_as_float(r[0]) + __uint_as_float(r[1]); }

#define SCHED() __builtin_amdgcn_sched_barrier(0)
template <int I> struct IC { static constexpr int v = I; };
template <int B, int E, class F> DI void sfor(F f) { if constexpr (B < E) { f(IC<B>{}); sfor<B + 1, E>(f); } }
typedef __amdgpu_buffer_rsrc_t rsrc_t;
DI rsrc_t mk_rsrc(const void* p, int bytes) { return __builtin_amdgcn_make_buffer_rsrc((void*)p, 0, bytes, 0x00020000); }
DI void dma16(rsrc_t r, LAS char* l, unsigned voff, int soff) {
  asm volatile("s_mov_b32 m0, %0\n\ts_nop 0\n\tbuffer_load_dwordx4 %1, %2, %3 offen lds"
               ::"s"((unsigned)(size_t)l), "v"(voff), "s"(r), "s"(soff) : "memory");
}
DI void l2touch(rsrc_t r, unsigned voff, int soff) {
  asm volatile("buffer_load_dword v255, %0, %1, %2 offen" ::"v"(voff), "s"(r), "s"(soff) : "memory", "v255");
}
template <int NM, int NR, int RPM, int NG, class M, class R, class G>
DI void mgroup(M mma, R rd, G gl) {
  sfor<0, NM>([&](auto i) {
    constexpr int I = decltype(i)::v;
    mma(i); SCHED();
    sfor<0, RPM>([&](auto j) { constexpr int X = I * RPM + decltype(j)::v; if constexpr (X < NR) rd(IC<X>{}); });
    if constexpr (I >= NM - NG) gl(IC<I - (NM - NG)>{});
    SCHED();
  });
}

template <bool NORM> DI void dual_exp(const f32x16& x, f32x16& pp, f32x16& pm) {
  float mx = fmaxf(x[0], x[1]), mn = fminf(x[0], x[1]);
#pragma unroll
  for (int i = 2; i < 16; i += 2) { mx = fmaxf(fmaxf(mx, x[i]), x[i + 1]); mn = fminf(fminf(mn, x[i]), x[i + 1]); }
  mx = hmax(mx); mn = hmin(mn);
  const float L2E = 1.4426950408889634f;
  const f32x2 l2 = {L2E, L2E}, nmx = {-mx * L2E, -mx * L2E}, pmn = {mn * L2E, mn * L2E};
  f32x2 sp = {0.f, 0.f}, sm = {0.f, 0.f};
#pragma unroll
  for (int i = 0; i < 16; i += 2) {
    const f32x2 xi = {x[i], x[i + 1]};
    const f32x2 a = __builtin_elementwise_fma(xi, l2, nmx), b = __builtin_elementwise_fma(xi, -l2, pmn);
    const f32x2 ea = {__builtin_amdgcn_exp2f(a[0]), __builtin_amdgcn_exp2f(a[1])};
    const f32x2 eb = {__builtin_amdgcn_exp2f(b[0]), __builtin_amdgcn_exp2f(b[1])};
    pp[i] = ea[0]; pp[i + 1] = ea[1]; pm[i] = eb[0]; pm[i + 1] = eb[1];
    if constexpr (NORM) { sp += ea; sm += eb; }
  }
  if constexpr (NORM) {
    const float rp = __builtin_amdgcn_rcpf(hsum(sp[0] + sp[1])), rm = __builtin_amdgcn_rcpf(hsum(sm[0] + sm[1]));
    const f32x2 rp2 = {rp, rp}, rm2 = {rm, rm};
#pragma unroll
    for (int i = 0; i < 16; i += 2) {
      f32x2 a = {pp[i], pp[i + 1]}, b = {pm[i], pm[i + 1]};
      a *= rp2; b *= rm2;
      pp[i] = a[0]; pp[i + 1] = a[1]; pm[i] = b[0]; pm[i + 1] = b[1];
    }
  }
}

DI void poll_flag(GAS u64* p, u64 tag) {
#pragma unroll 1
  for (unsigned spins = 0; spins < SPIN_CAP; ++spins) {
    const u64 v = __hip_atomic_load(p, RLX_AGENT);
    if (__all(v == tag)) break;
    __builtin_amdgcn_s_sleep(1);
  }
}

template <bool IS_A>
DI void phase2(LAS char* sm, char* ws, const u16* __restrict__ W0, const u16* __restrict__ W1, const u16* __restrict__ Bx,
               const u16* __restrict__ Dskip, float* __restrict__ out, int rt2, int ct2, int wave, int lane, int rot, u64 tag,
               bool upd_epoch, unsigned (&g_st)[2]) {
  char* g_ws = ws;
  constexpr int NT = IS_A ? 16 : 32;
  constexpr int S = 4, SB = 32768;
  const int h = lane >> 5, kh = wave >> 2, wm = (wave >> 1) & 1, wn = wave & 1;
  const int r5 = 8 * (wave & 3) + (lane >> 3);
  const unsigned voffR = (unsigned)r5 * 2048 + (unsigned)(((lane & 7) ^ ((r5 >> 1) & 7)) * 16);
  const unsigned voffT = (unsigned)r5 * (IS_A ? 2048 : 4096) + (unsigned)(((lane & 7) ^ (((r5 >> 1) & 1) << 2)) * 16);
  const char* gAct = IS_A ? ws + WS_O + (size_t)rt2 * 128 * 2048 : ws + WS_H + (size_t)rt2 * 256;
  const int actBytes = IS_A ? 128 * 2048 : 2048 * 4096 - rt2 * 256;
  const char* gW0 = IS_A ? (const char*)W0 + (size_t)ct2 * 256 : (const char*)W0 + (size_t)ct2 * 128 * 2048;
  const char* gW1 = IS_A ? gW0 : (const char*)W1 + (size_t)ct2 * 128 * 2048;
  const int wBytes = IS_A ? 1024 * 2048 - ct2 * 256 : 128 * 2048;
  LAS char* dst0 = sm + (wave & 3) * 1024;
  auto issAct = [&](int i, int kt, int buf) {
    const rsrc_t r = mk_rsrc(gAct, kt < NT ? actBytes : 0);
    kt = (kt + rot) & (NT - 1);
    if constexpr (IS_A) dma16(r, dst0 + buf + i * 4096, voffR, kt * 128 + i * 32 * 2048);
    else dma16(r, dst0 + buf + i * 4096, voffT, (kt * 64 + (i & 1) * 32) * 4096 + (i >> 1) * 128);
  };
  auto issW = [&](int i, int kt, int buf) {
    const bool live = kt < NT;
    kt = (kt + rot) & (NT - 1);
    if constexpr (IS_A) dma16(mk_rsrc(gW0, live ? wBytes : 0), dst0 + buf + 16384 + i * 4096, voffT, (kt * 64 + (i & 1) * 32) * 2048 + (i >> 1) * 128);
    else dma16(mk_rsrc(kt < 16 ? gW0 : gW1, live ? wBytes : 0), dst0 + buf + 16384 + i * 4096, voffR, (kt & 15) * 128 + i * 32 * 2048);
  };
  if (wave < 4) sfor<0, S - 1>([&](auto s_) { constexpr int s = decltype(s_)::v; sfor<0, 4>([&](auto i) { issW(decltype(i)::v, s, s * SB); }); });
  if (wave == 4) {
    poll_flag(flagp(ws, (IS_A ? FL_O : FL_H) + rt2 * 1024 + (lane >> 2) * 64 + (lane & 3) * 8), tag);
    STAMP(18);
    __builtin_amdgcn_fence(__ATOMIC_ACQUIRE, "agent");
    wait_vm<0>();
    if (upd_epoch && lane == 0) __hip_atomic_store(flagp(ws, IS_A ? FL_EPOCH_A : FL_EPOCH_B), tag, RLX_AGENT);
  }
  cfence(); bar(); cfence();
  STAMP(19);
  if (wave < 4) {
    sfor<0, S - 1>([&](auto s_) { constexpr int s = decltype(s_)::v; sfor<0, 4>([&](auto i) { issAct(decltype(i)::v, s, s * SB); }); });
  } else {
    const rsrc_t r = mk_rsrc(gAct, actBytes);
    const int j = ct2 & 3, w4 = wave & 3;
    if constexpr (IS_A) {
#pragma unroll
      for (int i = 0; i < 2; ++i) {
        const int kt = (4 * (2 * i + (w4 >> 1)) + j + rot) & 15;
        l2touch(r, (unsigned)(lane + 64 * (w4 & 1)) * 2048, kt * 128);
      }
    } else {
#pragma unroll
      for (int i = 0; i < 4; ++i) {
        const int kt = (4 * (2 * i + (w4 >> 1)) + j + rot) & 31;
        l2touch(r, (unsigned)lane * 4096 + (unsigned)(w4 & 1) * 128, kt * 64 * 4096);
      }
    }
  }
  const int xs = (lane >> 1) & 7;
  const int q = (lane & 15) >> 2, p4 = lane & 3, blk = (lane >> 4) & 1;
  int rofs[2], tofs[2];
#pragma unroll
  for (int s = 0; s < 2; ++s) {
    rofs[s] = (IS_A ? 64 * wm * 128 : 16384 + 64 * wn * 128) + (lane & 31) * 128 + 16 * ((2 * (2 * kh + s) + h) ^ xs);
    tofs[s] = (IS_A ? 16384 + wn * 8192 + (32 * kh + 16 * h + q) * 128 : wm * 8192 + (32 * kh + 8 * h + q) * 128) + 32 * blk + 8 * p4 +
              ((64 * s) ^ (64 * ((q >> 1) & 1)));
  }
  constexpr int TS = IS_A ? 512 : 2048, TT = IS_A ? 1024 : 512;
  f32x16 acc[2][2];
#pragma unroll
  for (int i = 0; i < 2; ++i)
#pragma unroll
    for (int j = 0; j < 2; ++j)
#pragma unroll
      for (int e = 0; e < 16; ++e) acc[i][j][e] = 0.f;
  {
    bf16x8 fr[2][2];
    s16x4 tl[2][2][2];
#pragma unroll
    for (int i = 0; i < 2; ++i) { fr[1][i] = zero8(); tl[1][i][0] = (s16x4){0, 0, 0, 0}; tl[1][i][1] = (s16x4){0, 0, 0, 0}; }
    auto mma = [&](auto B_, auto I) {
      constexpr int B = decltype(B_)::v, i = decltype(I)::v, ri = i >> 1, ti = i & 1;
      if constexpr (IS_A) acc[ri][ti] = MFMA32(fr[B][ri], cat8(tl[B][ti][0], tl[B][ti][1]), acc[ri][ti]);
      else acc[ti][ri] = MFMA32(cat8(tl[B][ti][0], tl[B][ti][1]), fr[B][ri], acc[ti][ri]);
    };
    auto rdf = [&](auto B_, auto S_, auto X, LAS char* sb) {
      constexpr int B = decltype(B_)::v, s = decltype(S_)::v, x = decltype(X)::v;
      if constexpr (x == 0) fr[B][0] = lds_b128(sb + rofs[s]);
      else if constexpr (x < 5) tl[B][(x - 1) >> 1][(x - 1) & 1] = lds_tr(sb + tofs[(x - 1) >> 1] + s * TS + ((x - 1) & 1) * TT);
      else fr[B][1] = lds_b128(sb + rofs[s] + 4096);
    };
    auto loop = [&](auto LD_) {
      constexpr bool LD = decltype(LD_)::v;
      int rd = 0, wr = (S - 1) * SB;
#pragma unroll 1
      for (int t = 0; t < NT; ++t) {
        if constexpr (LD) {
          if (t == 0) wait_vm<8>(); else if (t == 1) wait_vm<12>(); else wait_vm<16>();
        }
        wait_lgkm0(); bar(); cfence(); SCHED();
        LAS char* sb = sm + rd;
        mgroup<4, 6, 2, LD ? 4 : 0>([&](auto i) { mma(IC<1>{}, i); }, [&](auto x) { rdf(IC<0>{}, IC<0>{}, x, sb); },
                                    [&](auto g) { issAct(decltype(g)::v, t + S - 1, wr); });
        mgroup<4, 6, 2, LD ? 4 : 0>([&](auto i) { mma(IC<0>{}, i); }, [&](auto x) { rdf(IC<1>{}, IC<1>{}, x, sb); },
                                    [&](auto g) { issW(decltype(g)::v, t + S - 1, wr); });
        rd += SB; if (rd == S * SB) rd = 0;
        wr += SB; if (wr == S * SB) wr = 0;
      }
    };
    if (wave < 4) loop(IC<1>{}); else loop(IC<0>{});
    sfor<0, 4>([&](auto i) { mma(IC<1>{}, i); });
    wait_vm<0>();
  }
  STAMP(20);
  sync_lds();
  {
    LAS char* mine = sm + wave * 8192 + lane * 16;
#pragma unroll
    for (int j = 0; j < 2; ++j)
#pragma unroll
      for (int g = 0; g < 4; ++g) {
        f32x4 v;
#pragma unroll
        for (int e = 0; e < 4; ++e) v[e] = kh ? acc[0][j][4 * g + e] : acc[1][j][4 * g + e];
        *(LAS f32x4*)(mine + (j * 4 + g) * 1024) = v;
      }
  }
  const int row0 = rt2 * 128 + 64 * wm + 32 * kh + 4 * h;
  const int col0 = ct2 * 128 + 64 * wn + (lane & 31);
  float xd[2][16];
  if constexpr (!IS_A) {
#pragma unroll
    for (int j = 0; j < 2; ++j) {
      const float dsk = bf2f(Dskip[col0 + 32 * j]);
#pragma unroll
      for (int i = 0; i < 16; ++i) xd[j][i] = bf2f(Bx[(size_t)(row0 + (i & 3) + 8 * (i >> 2)) * 1024 + col0 + 32 * j]) * dsk;
    }
  }
  sync_lds();
  {
    LAS char* theirs = sm + (wave ^ 4) * 8192 + lane * 16;
    float* op = out + (size_t)row0 * 2048 + (IS_A ? 0 : 1024) + col0;
#pragma unroll
    for (int j = 0; j < 2; ++j)
#pragma unroll
      for (int g = 0; g < 4; ++g) {
        f32x4 v = *(LAS f32x4*)(theirs + (j * 4 + g) * 1024);
#pragma unroll
        for (int e = 0; e < 4; ++e) {
          float r = v[e] + (kh ? acc[1][j][4 * g + e] : acc[0][j][4 * g + e]);
          if constexpr (!IS_A) r += xd[j][4 * g + e];
          op[(size_t)(e + 8 * g) * 2048 + 32 * j] = r;
        }
      }
  }
  STAMP(21);
}

__global__ __launch_bounds__(512, 2) void fused_kernel(
    const u16* __restrict__ Ax, const u16* __restrict__ Wqkv, const u16* __restrict__ Wphi, const u16* __restrict__ Wo,
    const u16* __restrict__ Bx, const u16* __restrict__ Lre, const u16* __restrict__ Lim, const u16* __restrict__ Bre,
    const u16* __restrict__ Bim, const u16* __restrict__ Cre, const u16* __restrict__ Cim, const u16* __restrict__ Logdt,
    const u16* __restrict__ Dskip, float* __restrict__ out, char* ws) {
  __shared__ __attribute__((aligned(1024))) char smem[LDS_BYTES];
  LAS char* sm = (LAS char*)smem;
  char* g_ws = ws;
  unsigned g_st[2] = {0u, 0u};
  STAMP(0);
  const int tid = threadIdx.x, lane = tid & 63, wave = __builtin_amdgcn_readfirstlane(tid >> 6);
  const int b = blockIdx.x, xcd = b & 7, idx = b >> 3, i16 = idx >> 1;
  const bool isA = (idx & 1) == 0;
  const int rt = 4 * (xcd & 1) + (i16 & 3), cg = 4 * (xcd >> 1) + (i16 >> 2);
  const int rt2 = 4 * (xcd & 3) + (i16 & 3), ct2 = 4 * (xcd >> 2) + (i16 >> 2);
  const int h = lane >> 5, c32 = lane & 31, wm = wave >> 1, wn = wave & 1;
  const int rot1 = 2 * xcd;
  const int r5 = 8 * (wave & 3) + (lane >> 3);
  const unsigned rowchunk = (unsigned)(((lane & 7) ^ ((r5 >> 1) & 7)) * 16);
  const int xs = (lane >> 1) & 7;
  const int q = (lane & 15) >> 2, p4 = lane & 3, blk = (lane >> 4) & 1;
  int rofs[4];
#pragma unroll
  for (int s = 0; s < 4; ++s) rofs[s] = c32 * 128 + 16 * ((2 * s + h) ^ xs);
  LAS char* dst0 = sm + (wave & 3) * 1024;

  if (isA) {
    const int hd = 2 * cg + wn;
    constexpr int WR_OFF = 98304;
    const unsigned voffA = (unsigned)r5 * 2048 + rowchunk;
    const unsigned voffB = (unsigned)r5 * 6144 + (unsigned)(((lane & 7) ^ (((r5 >> 1) & 1) << 2)) * 16);
    const char* gA = (const char*)Ax + (size_t)rt * 256 * 2048;
    const char* gB = (const char*)Wqkv + (size_t)cg * 128;
    auto issX = [&](int i, int kt, int buf) {
      dma16(mk_rsrc(gA, kt < 16 ? 256 * 2048 : 0), dst0 + buf + i * 4096, voffA, ((kt + rot1) & 15) * 128 + i * 32 * 2048); };
    auto issW = [&](int j, int i, int kt, int u) {
      dma16(mk_rsrc(gB, kt < 16 ? 1024 * 6144 - cg * 128 : 0), dst0 + WR_OFF + (u & 7) * 8192 + i * 4096, voffB,
            (((kt + rot1) & 15) * 64 + i * 32) * 6144 + j * 2048); };
    const unsigned pfoff = (wave == 4) ? (unsigned)((xcd & 1) * 1024 + i16 * 64 + lane) * 2048
                                       : (unsigned)(4 * i16 + (lane < 48 ? lane / 12 : 0)) * 6144 + (unsigned)((lane % 12) >> 2) * 2048 +
                                             (unsigned)(4 * (xcd >> 1) + (lane & 3)) * 128;
    auto pf = [&](int kt) {
      const int k = (kt + rot1) & 15;
      if (wave == 4) l2touch(mk_rsrc(Ax, kt < 16 ? 2048 * 2048 : 0), pfoff, k * 128);
      else if (wave == 5) l2touch(mk_rsrc(Wqkv, kt < 16 ? 1024 * 6144 : 0), pfoff, k * 64 * 6144);
    };
    if (wave < 4) {
#pragma unroll
      for (int i = 0; i < 8; ++i) issX(i, 0, 0);
#pragma unroll
      for (int j = 0; j < 6; ++j) issW(j >> 1, j & 1, 0, j >> 1);
    } else {
#pragma unroll
      for (int k = 1; k < PFD; ++k) pf(k);
    }
    u64 epoch = __hip_atomic_load(flagp(ws, FL_EPOCH_A), RLX_AGENT);
    u16 wraw[16];
#pragma unroll
    for (int i = 0; i < 16; ++i) wraw[i] = Wphi[(size_t)hd * 1024 + ((i & 3) + 8 * (i >> 2) + 4 * h) * 32 + c32];
    cfence();
    if (wave < 4) {
#pragma unroll
      for (int i = 0; i < 8; ++i) issX(i, 1, 32768);
#pragma unroll
      for (int j = 0; j < 4; ++j) issW(j >> 1, j & 1, 1, 3 + (j >> 1));
    }
    const int tofs = WR_OFF + (8 * h + q) * 128 + 64 * (wn ^ ((q >> 1) & 1)) + 32 * blk + 8 * p4;
    f32x16 Qt[2], Kt[2], V[2];
#pragma unroll
    for (int m = 0; m < 2; ++m)
#pragma unroll
      for (int e = 0; e < 16; ++e) { Qt[m][e] = 0.f; Kt[m][e] = 0.f; V[m][e] = 0.f; }
    STAMP(1);
    {
      bf16x8 fx[2][2];
      s16x4 fw[2][3][2];
#pragma unroll
      for (int i = 0; i < 2; ++i) fx[1][i] = zero8();
#pragma unroll
      for (int j = 0; j < 3; ++j) { fw[1][j][0] = (s16x4){0, 0, 0, 0}; fw[1][j][1] = (s16x4){0, 0, 0, 0}; }
      auto mma = [&](auto B_, auto I) {
        constexpr int B = decltype(B_)::v, i = decltype(I)::v, m = i / 3, j = i % 3;
        const bf16x8 w = cat8(fw[B][j][0], fw[B][j][1]);
        if constexpr (j == 0) Qt[m] = MFMA32(w, fx[B][m], Qt[m]);
        else if constexpr (j == 1) Kt[m] = MFMA32(w, fx[B][m], Kt[m]);
        else V[m] = MFMA32(fx[B][m], w, V[m]);
      };
      auto loop = [&](auto LD_) {
        constexpr bool LD = decltype(LD_)::v;
        int xr = 0, xw = 65536, u0 = 0;
#pragma unroll 1
        for (int t = 0; t < 16; ++t) {
          if constexpr (LD) wait_vm<12>();
          wait_lgkm0(); bar(); cfence(); SCHED();
          LAS char* xb = sm + xr + wm * 8192;
          LAS char* wb[3] = {sm + tofs + (u0 & 7) * 8192, sm + tofs + ((u0 + 1) & 7) * 8192, sm + tofs + ((u0 + 2) & 7) * 8192};
          auto rdf = [&](auto B_, auto S_, auto X) {
            constexpr int B = decltype(B_)::v, s = decltype(S_)::v, x = decltype(X)::v;
            if constexpr (x == 0) fx[B][0] = lds_b128(xb + rofs[s]);
            else if constexpr (x < 7) fw[B][(x - 1) >> 1][(x - 1) & 1] = lds_tr(wb[(x - 1) >> 1] + s * 2048 + ((x - 1) & 1) * 512);
            else fx[B][1] = lds_b128(xb + rofs[s] + 4096);
          };
          auto gl = [&](auto G) {
            constexpr int g = decltype(G)::v;
            if constexpr (LD) {
              if constexpr (g < 2) issW(2, g, t + 1, u0 + 5);
              else if constexpr (g < 10) issX(g - 2, t + 2, xw);
              else if constexpr (g < 14) issW((g - 10) >> 1, g & 1, t + 2, u0 + 6 + ((g - 10) >> 1));
            } else {
              if constexpr (g == 0) pf(t + PFD);
            }
          };
          mgroup<6, 8, 2, 4>([&](auto i) { mma(IC<1>{}, i); }, [&](auto x) { rdf(IC<0>{}, IC<0>{}, x); }, [&](auto g) { gl(IC<0 + decltype(g)::v>{}); });
          mgroup<6, 8, 2, 4>([&](auto i) { mma(IC<0>{}, i); }, [&](auto x) { rdf(IC<1>{}, IC<1>{}, x); }, [&](auto g) { gl(IC<4 + decltype(g)::v>{}); });
          mgroup<6, 8, 2, 4>([&](auto i) { mma(IC<1>{}, i); }, [&](auto x) { rdf(IC<0>{}, IC<2>{}, x); }, [&](auto g) { gl(IC<8 + decltype(g)::v>{}); });
          mgroup<6, 8, 2, 4>([&](auto i) { mma(IC<0>{}, i); }, [&](auto x) { rdf(IC<1>{}, IC<3>{}, x); }, [&](auto g) { gl(IC<12 + decltype(g)::v>{}); });
          xr += 32768; if (xr == 98304) xr = 0;
          xw += 32768; if (xw == 98304) xw = 0;
          u0 = (u0 + 3) & 7;
        }
      };
      if (wave < 4) loop(IC<1>{}); else loop(IC<0>{});
      sfor<0, 6>([&](auto i) { mma(IC<1>{}, i); });
      wait_vm<0>();
    }
    asm volatile("" : "+v"(epoch));
    const u64 tag = epoch + 1ull;
    const unsigned tlo = (unsigned)tag, thi = (unsigned)(tag >> 32);
    {
      const rsrc_t r = mk_rsrc(Wo, 1024 * 2048);
      l2touch(r, (unsigned)(i16 * 64 + wave * 8 + (lane >> 3)) * 2048 + (unsigned)(lane & 7) * 128, (xcd >> 2) * 1024);
    }
    STAMP(2);
    sync_lds();
    STAMP(3);

    bf16x8 wf0, wf1;
    {
      s16x8 t0, t1;
#pragma unroll
      for (int j = 0; j < 8; ++j) { t0[j] = (short)wraw[j]; t1[j] = (short)wraw[8 + j]; }
      wf0 = __builtin_bit_cast(bf16x8, t0); wf1 = __builtin_bit_cast(bf16x8, t1);
    }
    f32x16 zero16;
#pragma unroll
    for (int e = 0; e < 16; ++e) zero16[e] = 0.f;
    constexpr int PH_PITCH = 136, PH_BYTES = 64 * PH_PITCH;
    LAS char* ph = sm + wave * PH_BYTES;
#pragma unroll
    for (int m = 0; m < 2; ++m) {
      f32x16 pk = MFMA32(wf0, pack8<0>(Kt[m]), zero16);
      pk = MFMA32(wf1, pack8<1>(Kt[m]), pk);
      f32x16 pp, pm;
      dual_exp<true>(pk, pp, pm);
      LAS char* rowp = ph + (32 * m + c32) * PH_PITCH + 8 * h;
#pragma unroll
      for (int g = 0; g < 4; ++g) {
        u32x2 a = {pk2(pp[4 * g], pp[4 * g + 1]), pk2(pp[4 * g + 2], pp[4 * g + 3])};
        u32x2 c = {pk2(pm[4 * g], pm[4 * g + 1]), pk2(pm[4 * g + 2], pm[4 * g + 3])};
        *(LAS u32x2*)(rowp + 16 * g) = a;
        *(LAS u32x2*)(rowp + 64 + 16 * g) = c;
      }
    }
    wait_lgkm0();
    STAMP(4);
    f32x16 KV[2], Z[2];
    KV[0] = zero16; KV[1] = zero16; Z[0] = zero16; Z[1] = zero16;
    {
      s16x8 o8;
#pragma unroll
      for (int j = 0; j < 8; ++j) o8[j] = (short)0x3f80;
      const bf16x8 ones = __builtin_bit_cast(bf16x8, o8);
      LAS char* tp = ph + (4 * h + q) * PH_PITCH + 32 * blk + 8 * p4;
#pragma unroll
      for (int m = 0; m < 2; ++m) {
#pragma unroll
        for (int s = 0; s < 2; ++s) {
          const bf16x8 vb = s ? pack8<1>(V[m]) : pack8<0>(V[m]);
          LAS char* t0 = tp + (32 * m + 16 * s) * PH_PITCH;
#pragma unroll
          for (int fs = 0; fs < 2; ++fs) {
            const bf16x8 af = cat8(lds_tr(t0 + 64 * fs), lds_tr(t0 + 8 * PH_PITCH + 64 * fs));
            KV[fs] = MFMA32(af, vb, KV[fs]);
            Z[fs] = MFMA32(af, ones, Z[fs]);
          }
        }
      }
    }
    constexpr int RED_OFF = 8 * PH_BYTES;
    constexpr int RED_W = 8192 + 256;
    {
      LAS char* rw = sm + RED_OFF + wave * RED_W;
#pragma unroll
      for (int fs = 0; fs < 2; ++fs)
#pragma unroll
        for (int g = 0; g < 4; ++g) {
          f32x4 v = {KV[fs][4 * g], KV[fs][4 * g + 1], KV[fs][4 * g + 2], KV[fs][4 * g + 3]};
          *(LAS f32x4*)(rw + (fs * 4 + g) * 1024 + lane * 16) = v;
        }
      if (c32 == 0) {
#pragma unroll
        for (int fs = 0; fs < 2; ++fs)
#pragma unroll
          for (int g = 0; g < 4; ++g) {
            f32x4 z = {Z[fs][4 * g], Z[fs][4 * g + 1], Z[fs][4 * g + 2], Z[fs][4 * g + 3]};
            *(LAS f32x4*)(rw + 8192 + ((fs * 2 + h) * 4 + g) * 16) = z;
          }
      }
    }
    STAMP(5);
    sync_lds();
    STAMP(6);
    const rsrc_t kvrs = mk_rsrc(ws + WS_KVP + (size_t)cg * 8 * KVP_BYTES, 8 * KVP_BYTES);
    {
#pragma unroll
      for (int hh = 0; hh < 2; ++hh) {
        f32x4 s = {0.f, 0.f, 0.f, 0.f};
#pragma unroll
        for (int w = 0; w < 4; ++w) s += *(LAS f32x4*)(sm + RED_OFF + (2 * w + hh) * RED_W + tid * 16);
        const u32x4 gr = {pk2(s[0], s[1]), tlo, pk2(s[2], s[3]), thi};
        __builtin_amdgcn_raw_buffer_store_b128(gr, kvrs, rt * KVP_BYTES + hh * 8448 + tid * 16, 0, 16);
      }
      if (tid < 32) {
        const int hh = tid >> 4;
        f32x4 s = {0.f, 0.f, 0.f, 0.f};
#pragma unroll
        for (int w = 0; w < 4; ++w) s += *(LAS f32x4*)(sm + RED_OFF + (2 * w + hh) * RED_W + 8192 + (tid & 15) * 16);
        const u32x4 gr = {pk2(s[0], s[1]), tlo, pk2(s[2], s[3]), thi};
        __builtin_amdgcn_raw_buffer_store_b128(gr, kvrs, rt * KVP_BYTES + hh * 8448 + 8192 + (tid & 15) * 16, 0, 16);
      }
      if (tid == 448) __hip_atomic_store(flagp(ws, FL_KV + (cg * 8 + rt) * 128), tag, RLX_AGENT);
    }
    STAMP(9);
    bf16x8 fq[2][4];
#pragma unroll
    for (int m = 0; m < 2; ++m) {
      f32x16 pq = MFMA32(wf0, pack8<0>(Qt[m]), zero16);
      pq = MFMA32(wf1, pack8<1>(Qt[m]), pq);
      f32x16 pp, pm;
      dual_exp<false>(pq, pp, pm);
      fq[m][0] = pack8<0>(pp); fq[m][1] = pack8<1>(pp); fq[m][2] = pack8<0>(pm); fq[m][3] = pack8<1>(pm);
    }
    asm volatile("" : "+v"(fq[0][0]), "+v"(fq[0][1]), "+v"(fq[0][2]), "+v"(fq[0][3]), "+v"(fq[1][0]), "+v"(fq[1][1]), "+v"(fq[1][2]), "+v"(fq[1][3]));
    STAMP(10);
    if (wave == 0) poll_flag(flagp(ws, FL_KV + (cg * 8 + (lane & 7)) * 128), tag);
    STAMP(11);
    cfence(); bar(); cfence();
    STAMP(12);
    constexpr int KVF_OFF = 0;
    {
      const int zoff = 8192 + (tid >> 4 & 1) * 8448 + (tid & 15) * 16;
      u32x4 v[3][8];
#pragma unroll 1
      for (unsigned spins = 0; spins < SPIN_CAP; ++spins) {
#pragma unroll
        for (int r = 0; r < 8; ++r) {
          v[0][r] = __builtin_amdgcn_raw_buffer_load_b128(kvrs, tid * 16, r * KVP_BYTES, 16);
          v[1][r] = __builtin_amdgcn_raw_buffer_load_b128(kvrs, 8448 + tid * 16, r * KVP_BYTES, 16);
        }
        if (wave == 0) {
#pragma unroll
          for (int r = 0; r < 8; ++r) v[2][r] = __builtin_amdgcn_raw_buffer_load_b128(kvrs, zoff, r * KVP_BYTES, 16);
        } else {
#pragma unroll
          for (int r = 0; r < 8; ++r) v[2][r] = v[0][r];
        }
        unsigned bad = 0;
#pragma unroll
        for (int a = 0; a < 3; ++a)
#pragma unroll
          for (int r = 0; r < 8; ++r) bad |= (v[a][r][1] ^ tlo) | (v[a][r][3] ^ thi);
        if (__all(bad == 0)) break;
        __builtin_amdgcn_s_sleep(2);
      }
#pragma unroll
      for (int a = 0; a < 3; ++a) {
        f32x4 s = {0.f, 0.f, 0.f, 0.f};
#pragma unroll
        for (int r = 0; r < 8; ++r) s += (f32x4){bflo(v[a][r][0]), bfhi(v[a][r][0]), bflo(v[a][r][2]), bfhi(v[a][r][2])};
        const u32x2 w = {pk2(s[0], s[1]), pk2(s[2], s[3])};
        if (a < 2) {
          *(LAS u32x2*)(sm + KVF_OFF + a * 4096 + (wave >> 1) * 1024 + lane * 16 + (wave & 1) * 8) = w;
        } else if (tid < 32) {
          const int hh = tid >> 4, z = tid & 15, fs = z >> 3, zh = (z >> 2) & 1, g = z & 3;
          *(LAS u32x2*)(sm + KVF_OFF + 8192 + hh * 128 + ((fs * 2 + (g >> 1)) * 2 + zh) * 16 + (g & 1) * 8) = w;
        }
      }
    }
    STAMP(13);
    sync_lds();
    STAMP(14);
    constexpr int OI_OFF = 16384, OI_PITCH = 144;
    {
      bf16x8 kf[4], zf[4];
      s16x8 o8;
#pragma unroll
      for (int j = 0; j < 8; ++j) o8[j] = (short)0x3f80;
#pragma unroll
      for (int ks = 0; ks < 4; ++ks) {
        kf[ks] = lds_b128(sm + KVF_OFF + wn * 4096 + ks * 1024 + lane * 16);
        const bf16x8 z = lds_b128(sm + KVF_OFF + 8192 + wn * 128 + (ks * 2 + h) * 16);
        zf[ks] = (lane & 1) ? __builtin_bit_cast(bf16x8, o8) : z;
      }
#pragma unroll
      for (int m = 0; m < 2; ++m) {
        f32x16 np = MFMA32(kf[0], fq[m][0], zero16), nm = MFMA32(kf[2], fq[m][2], zero16);
        f32x16 dp = MFMA32(zf[0], fq[m][0], zero16), dm = MFMA32(zf[2], fq[m][2], zero16);
        np = MFMA32(kf[1], fq[m][1], np); nm = MFMA32(kf[3], fq[m][3], nm);
        dp = MFMA32(zf[1], fq[m][1], dp); dm = MFMA32(zf[3], fq[m][3], dm);
        const float rp = __builtin_amdgcn_rcpf(dp[1]), rm = __builtin_amdgcn_rcpf(dm[1]);
        const float rinv = 1.0f / (dp[0] * rp + dm[0] * rm + 1e-6f);
        const float cp = rp * rinv, cm = rm * rinv;
        f32x16 o;
#pragma unroll
        for (int e = 0; e < 16; ++e) o[e] = np[e] * cp + nm[e] * cm;
        LAS char* op = sm + OI_OFF + (64 * wm + 32 * m + c32) * OI_PITCH + wn * 64 + h * 32;
        *(LAS bf16x8*)op = pack8<0>(o);
        *(LAS bf16x8*)(op + 16) = pack8<1>(o);
      }
    }
    sync_lds();
    {
      const rsrc_t ors = mk_rsrc(ws + WS_O, 2048 * 2048);
      const int row = 32 * wave + (lane >> 3);
#pragma unroll
      for (int j = 0; j < 4; ++j) {
        const u32x4 v = *(LAS u32x4*)(sm + OI_OFF + (row + 8 * j) * OI_PITCH + (lane & 7) * 16);
        __builtin_amdgcn_raw_buffer_store_b128(v, ors, (rt * 256 + row + 8 * j) * 2048 + cg * 128 + (lane & 7) * 16, 0, 16);
      }
    }
    STAMP(15);
    wait_vm<0>();
    STAMP(16);
    if (lane == 0) __hip_atomic_store(flagp(ws, FL_O + ((2 * rt + (wave >> 2)) * 16 + cg) * 64 + (wave & 3) * 8), tag, RLX_AGENT);
    sync_lds();
    STAMP(17);
    phase2<true>(sm, ws, Wo, Wo, Bx, Dskip, out, rt2, ct2, wave, lane, rot1, tag, b == 0, g_st);
  } else {
    constexpr int SB = 49152, S = 3;
    constexpr int TE_OFF = S * SB;
    constexpr int TG_OFF = TE_OFF + 4096;
    constexpr int TC_OFF = TG_OFF + 4096;
    const int tloc = 32 * ((r5 >> 2) & 1) + 16 * (r5 & 1) + ((r5 & 3) >> 1) + 2 * (r5 >> 3);
    const unsigned voffA = (unsigned)tloc * 2048 + rowchunk;
    const unsigned voffB = (unsigned)r5 * 2048 + rowchunk;
    const char* gA = (const char*)Bx + (size_t)rt * 256 * 2048;
    const char* gBr = (const char*)Bre + (size_t)cg * 64 * 2048;
    const char* gBi = (const char*)Bim + (size_t)cg * 64 * 2048;
    auto issue = [&](auto I, int kt, int buf) {
      constexpr int i = decltype(I)::v;
      const bool live = kt < 16;
      kt = (kt + rot1) & 15;
      if constexpr (i < 8) dma16(mk_rsrc(gA, live ? 256 * 2048 : 0), dst0 + buf + i * 4096, voffA, kt * 128 + ((i >> 1) * 64 + (i & 1) * 8) * 2048);
      else dma16(mk_rsrc(i < 10 ? gBr : gBi, live ? 64 * 2048 : 0), dst0 + buf + i * 4096, voffB, kt * 128 + (i & 1) * 32 * 2048);
    };
    const unsigned pfoff = (wave == 4) ? (unsigned)((xcd & 1) * 1024 + i16 * 64 + lane) * 2048
                                       : (unsigned)((xcd >> 1) * 256 + i16 * 16 + (lane & 15)) * 2048;
    auto pf = [&](int kt) {
      const int k = (kt + rot1) & 15;
      if (wave == 4) l2touch(mk_rsrc(Bx, kt < 16 ? 2048 * 2048 : 0), pfoff, k * 128);
      else if (wave == 5) l2touch(mk_rsrc(Bre, kt < 16 ? 1024 * 2048 : 0), pfoff, k * 128);
      else if (wave == 6) l2touch(mk_rsrc(Bim, kt < 16 ? 1024 * 2048 : 0), pfoff, k * 128);
    };
    if (wave < 4) {
      sfor<0, 12>([&](auto i) { issue(i, 0, 0); });
      sfor<0, 12>([&](auto i) { issue(i, 1, SB); });
    } else {
#pragma unroll
      for (int k = 2; k < PFD; ++k) pf(k);
    }
    u64 epoch = __hip_atomic_load(flagp(ws, FL_EPOCH_B), RLX_AGENT);
    if (wave == 7) {
      const int ch = 64 * cg + lane;
      const float lamr = bf2f(Lre[ch]), lami = bf2f(Lim[ch]);
      const float dt = expf(bf2f(Logdt[ch]));
      const float zr = dt * lamr, zi = dt * lami;
      float sn, cs;
      sincosf(zi, &sn, &cs);
      const float mag = expf(zr);
      const float lr = mag * cs, li = mag * sn;
      const float nr = lr - 1.0f, ni = li, dd = 2.0f / (lamr * lamr + lami * lami);
      const float c2r = (nr * lamr + ni * lami) * dd, c2i = (ni * lamr - nr * lami) * dd;
      float pr = lr, pi = li;
#pragma unroll
      for (int i = 0; i < 4; ++i) { const float t = pr * pr - pi * pi; pi = 2.0f * pr * pi; pr = t; }
      const float l16r = pr, l16i = pi;
      { const float t = pr * pr - pi * pi; pi = 2.0f * pr * pi; pr = t; }
      const float l32r = pr, l32i = pi;
#pragma unroll
      for (int i = 0; i < 3; ++i) { const float t = pr * pr - pi * pi; pi = 2.0f * pr * pi; pr = t; }
      LAS f32x4* tc = (LAS f32x4*)(sm + TC_OFF + lane * 48);
      tc[0] = (f32x4){lr, li, c2r, c2i}; tc[1] = (f32x4){l16r, l16i, l32r, l32i}; tc[2] = (f32x4){pr, pi, 0.f, 0.f};
    }
    f32x16 Ur[2], Ui[2];
#pragma unroll
    for (int m = 0; m < 2; ++m)
#pragma unroll
      for (int e = 0; e < 16; ++e) { Ur[m][e] = 0.f; Ui[m][e] = 0.f; }
    STAMP(1);
    {
      bf16x8 fx[2][2], fb[2][2];
#pragma unroll
      for (int i = 0; i < 2; ++i) { fx[1][i] = zero8(); fb[1][i] = zero8(); }
      auto mma = [&](auto B_, auto I) {
        constexpr int B = decltype(B_)::v, i = decltype(I)::v;
        if constexpr (i == 0) Ur[0] = MFMA32(fx[B][0], fb[B][0], Ur[0]);
        else if constexpr (i == 1) Ui[0] = MFMA32(fx[B][0], fb[B][1], Ui[0]);
        else if constexpr (i == 2) Ur[1] = MFMA32(fx[B][1], fb[B][0], Ur[1]);
        else Ui[1] = MFMA32(fx[B][1], fb[B][1], Ui[1]);
      };
      auto loop = [&](auto LD_) {
        constexpr bool LD = decltype(LD_)::v;
        int rd = 0, wr = 2 * SB;
#pragma unroll 1
        for (int t = 0; t < 16; ++t) {
          if constexpr (LD) wait_vm<12>();
          wait_lgkm0(); bar(); cfence(); SCHED();
          LAS char* xb = sm + rd + wm * 8192;
          LAS char* bb = sm + rd + 32768 + wn * 4096;
          auto rdf = [&](auto B_, auto S_, auto X) {
            constexpr int B = decltype(B_)::v, s = decltype(S_)::v, x = decltype(X)::v;
            if constexpr (x == 0) fx[B][0] = lds_b128(xb + rofs[s]);
            else if constexpr (x == 1) fb[B][0] = lds_b128(bb + rofs[s]);
            else if constexpr (x == 2) fb[B][1] = lds_b128(bb + rofs[s] + 8192);
            else fx[B][1] = lds_b128(xb + rofs[s] + 4096);
          };
          auto gl = [&](auto G) {
            constexpr int g = decltype(G)::v;
            if constexpr (LD) issue(G, t + 2, wr);
            else if constexpr (g == 0) pf(t + PFD);
          };
          mgroup<4, 4, 2, 3>([&](auto i) { mma(IC<1>{}, i); }, [&](auto x) { rdf(IC<0>{}, IC<0>{}, x); }, [&](auto g) { gl(IC<0 + decltype(g)::v>{}); });
          mgroup<4, 4, 2, 3>([&](auto i) { mma(IC<0>{}, i); }, [&](auto x) { rdf(IC<1>{}, IC<1>{}, x); }, [&](auto g) { gl(IC<3 + decltype(g)::v>{}); });
          mgroup<4, 4, 2, 3>([&](auto i) { mma(IC<1>{}, i); }, [&](auto x) { rdf(IC<0>{}, IC<2>{}, x); }, [&](auto g) { gl(IC<6 + decltype(g)::v>{}); });
          mgroup<4, 4, 2, 3>([&](auto i) { mma(IC<0>{}, i); }, [&](auto x) { rdf(IC<1>{}, IC<3>{}, x); }, [&](auto g) { gl(IC<9 + decltype(g)::v>{}); });
          rd += SB; if (rd == S * SB) rd = 0;
          wr += SB; if (wr == S * SB) wr = 0;
        }
      };
      if (wave < 4) loop(IC<1>{}); else loop(IC<0>{});
      sfor<0, 4>([&](auto i) { mma(IC<1>{}, i); });
      wait_vm<0>();
    }
    asm volatile("" : "+v"(epoch));
    const u64 tag = epoch + 1ull;
    const unsigned tlo = (unsigned)tag, thi = (unsigned)(tag >> 32);
    {
      const unsigned off = (unsigned)((xcd >> 2) * 512 + i16 * 32 + wave * 4 + (lane >> 4)) * 2048 + (unsigned)(lane & 15) * 128;
      l2touch(mk_rsrc(Cre, 1024 * 2048), off, 0);
      l2touch(mk_rsrc(Cim, 1024 * 2048), off, 0);
    }
    STAMP(2);
    const int seg = 2 * wm + h, chl = 32 * wn + c32;
    const f32x4 k0 = *(LAS f32x4*)(sm + TC_OFF + chl * 48), k1 = *(LAS f32x4*)(sm + TC_OFF + chl * 48 + 16), k2 = *(LAS f32x4*)(sm + TC_OFF + chl * 48 + 32);
    const f32x2 lr2 = {k0[0], k0[0]}, li2 = {k0[1], k0[1]}, cr2 = {k0[2], k0[2]}, ci2 = {k0[3], k0[3]};
    const float l16r = k1[0], l16i = k1[1], l32r = k1[2], l32i = k1[3], l256r = k2[0], l256i = k2[1];
    f32x2 er = {0.f, 0.f}, ei = {0.f, 0.f};
#pragma unroll
    for (int m = 0; m < 2; ++m)
#pragma unroll
      for (int i = 0; i < 16; i += 2) {
        const f32x2 ur = {Ur[m][i], Ur[m][i + 1]}, ui = {Ui[m][i], Ui[m][i + 1]};
        const f32x2 nr = __builtin_elementwise_fma(lr2, er, __builtin_elementwise_fma(-li2, ei, ur));
        const f32x2 ni = __builtin_elementwise_fma(lr2, ei, __builtin_elementwise_fma(li2, er, ui));
        er = nr; ei = ni;
      }
    const float e0r = er[0], e0i = ei[0];
    *(LAS f32x2*)(sm + TE_OFF + (seg * 64 + chl) * 8) =
        (f32x2){fmaf(l16r, e0r, fmaf(-l16i, e0i, er[1])), fmaf(l16r, e0i, fmaf(l16i, e0r, ei[1]))};
    sync_lds();
    f32x2 E[8];
#pragma unroll
    for (int s = 0; s < 8; ++s) E[s] = *(LAS f32x2*)(sm + TE_OFF + (s * 64 + chl) * 8);
    const rsrc_t agrs = mk_rsrc(ws + WS_AGG + (size_t)cg * 8192, 8192);
    if (wm == 0) {
      float tr = 0.f, ti = 0.f;
#pragma unroll
      for (int s = 0; s < 8; ++s) { const float nr = fmaf(l32r, tr, fmaf(-l32i, ti, E[s][0])), ni = fmaf(l32r, ti, fmaf(l32i, tr, E[s][1])); tr = nr; ti = ni; }
      if (h == 0) {
        const u32x4 gr = {__float_as_uint(tr), tlo, __float_as_uint(ti), thi};
        __builtin_amdgcn_raw_buffer_store_b128(gr, agrs, (rt * 64 + chl) * 16, 0, 16);
      }
      STAMP(3);
      u32x4 gr[4];
#pragma unroll 1
      for (unsigned spins = 0; spins < SPIN_CAP; ++spins) {
        unsigned bad = 0;
#pragma unroll
        for (int k = 0; k < 4; ++k) {
          const int r = 2 * k + h;
          gr[k] = __builtin_amdgcn_raw_buffer_load_b128(agrs, (r * 64 + chl) * 16, 0, 16);
        }
#pragma unroll
        for (int k = 0; k < 4; ++k) if (2 * k + h < rt) bad |= (gr[k][1] ^ tlo) | (gr[k][3] ^ thi);
        if (__all(bad == 0)) break;
        __builtin_amdgcn_s_sleep(1);
      }
#pragma unroll
      for (int k = 0; k < 4; ++k)
        *(LAS f32x2*)(sm + TG_OFF + ((2 * k + h) * 64 + chl) * 8) = (f32x2){__uint_as_float(gr[k][0]), __uint_as_float(gr[k][2])};
    }
    STAMP(4);
    sync_lds();
    float sr = 0.f, si = 0.f;
#pragma unroll 1
    for (int r = 0; r < rt; ++r) {
      const f32x2 g = *(LAS f32x2*)(sm + TG_OFF + (r * 64 + chl) * 8);
      const float nr = fmaf(l256r, sr, fmaf(-l256i, si, g[0])), ni = fmaf(l256r, si, fmaf(l256i, sr, g[1]));
      sr = nr; si = ni;
    }
#pragma unroll
    for (int s = 0; s < 7; ++s) {
      const float nr = fmaf(l32r, sr, fmaf(-l32i, si, E[s][0])), ni = fmaf(l32r, si, fmaf(l32i, sr, E[s][1]));
      if (s < seg) { sr = nr; si = ni; }
    }
    er = (f32x2){sr, fmaf(l16r, sr, fmaf(-l16i, si, e0r))};
    ei = (f32x2){si, fmaf(l16r, si, fmaf(l16i, sr, e0i))};
    constexpr int HI_PITCH = 528;
    {
      LAS char* hp = sm + chl * HI_PITCH + 128 * wm + 64 * h;
#pragma unroll
      for (int m = 0; m < 2; ++m) {
        f32x2 hr[8], hi[8];
#pragma unroll
        for (int i = 0; i < 16; i += 2) {
          const f32x2 ur = {Ur[m][i], Ur[m][i + 1]}, ui = {Ui[m][i], Ui[m][i + 1]};
          const f32x2 nr = __builtin_elementwise_fma(lr2, er, __builtin_elementwise_fma(-li2, ei, ur));
          const f32x2 ni = __builtin_elementwise_fma(lr2, ei, __builtin_elementwise_fma(li2, er, ui));
          er = nr; ei = ni;
          hr[i >> 1] = __builtin_elementwise_fma(cr2, er, -(ci2 * ei));
          hi[i >> 1] = -__builtin_elementwise_fma(cr2, ei, ci2 * er);
        }
#pragma unroll
        for (int c = 0; c < 2; ++c) {
          const u32x4 wr_ = {pk2(hr[0][c], hr[1][c]), pk2(hr[2][c], hr[3][c]), pk2(hr[4][c], hr[5][c]), pk2(hr[6][c], hr[7][c])};
          const u32x4 wi_ = {pk2(hi[0][c], hi[1][c]), pk2(hi[2][c], hi[3][c]), pk2(hi[4][c], hi[5][c]), pk2(hi[6][c], hi[7][c])};
          *(LAS u32x4*)(hp + 32 * c + 16 * m) = wr_;
          *(LAS u32x4*)(hp + 64 * HI_PITCH + 32 * c + 16 * m) = wi_;
        }
      }
    }
    STAMP(5);
    sync_lds();
    STAMP(6);
    {
      const rsrc_t hrs = mk_rsrc(ws + WS_H, 2048 * 4096);
      const int kr = 32 * (wave & 3) + (lane >> 4), tb = (wave >> 2) * 256 + (lane & 15) * 16;
#pragma unroll
      for (int j = 0; j < 8; ++j) {
        const int k = kr + 4 * j;
        const u32x4 v = *(LAS u32x4*)(sm + k * HI_PITCH + tb);
        __builtin_amdgcn_raw_buffer_store_b128(v, hrs, ((k >> 6) * 1024 + cg * 64 + (k & 63)) * 4096 + rt * 512 + tb, 0, 16);
      }
    }
    STAMP(15);
    wait_vm<0>();
    STAMP(16);
    if (lane == 0) __hip_atomic_store(flagp(ws, FL_H + ((2 * rt + (wave >> 2)) * 16 + cg) * 64 + (wave & 3) * 8), tag, RLX_AGENT);
    sync_lds();
    STAMP(17);
    phase2<false>(sm, ws, Cre, Cim, Bx, Dskip, out, rt2, ct2, wave, lane, rot1 * 2, tag, rt2 == 15 && ct2 == 0, g_st);
  }
}

extern "C" void kernel_launch(void* const* d_in, const int* in_sizes, int n_in, void* d_out, int out_size, void* d_ws,
                              size_t ws_size, hipStream_t stream) {
  fused_kernel<<<256, 512, 0, stream>>>((const u16*)d_in[0], (const u16*)d_in[1], (const u16*)d_in[2], (const u16*)d_in[3],
                                        (const u16*)d_in[4], (const u16*)d_in[5], (const u16*)d_in[6], (const u16*)d_in[7],
                                        (const u16*)d_in[8], (const u16*)d_in[9], (const u16*)d_in[10], (const u16*)d_in[11],
                                        (const u16*)d_in[12], (float*)d_out, (char*)d_ws);
}
```

```cpp
#include <hip/hip_runtime.h>
#include <stdint.h>

#define LAS __attribute__((address_space(3)))
#define GAS __attribute__((address_space(1)))
#define DI __device__ __forceinline__

typedef unsigned short u16;
typedef unsigned long long u64;
typedef __bf16 bf16x8 __attribute__((ext_vector_type(8)));
typedef __bf16 bf16x2 __attribute__((ext_vector_type(2)));
typedef short s16x4 __attribute__((ext_vector_type(4)));
typedef short s16x8 __attribute__((ext_vector_type(8)));
typedef float f32x2 __attribute__((ext_vector_type(2)));
typedef float f32x4 __attribute__((ext_vector_type(4)));
typedef float f32x16 __attribute__((ext_vector_type(16)));
typedef unsigned u32x2 __attribute__((ext_vector_type(2)));
typedef unsigned u32x4 __attribute__((ext_vector_type(4)));

#define MFMA32(a, b, c) __builtin_amdgcn_mfma_f32_32x32x16_bf16((a), (b), (c), 0, 0, 0)
#define RLX_AGENT __ATOMIC_RELAXED, __HIP_MEMORY_SCOPE_AGENT

constexpr size_t WS_O    = 0;
constexpr size_t WS_H    = 4u << 20;
constexpr size_t WS_KVP  = 12u << 20;
constexpr int    KVP_BYTES = 8448;
constexpr size_t WS_AGG  = 15u << 20;
constexpr size_t WS_FLAG = 16u << 20;
constexpr int FL_EPOCH_A = 0, FL_EPOCH_B = 256;
constexpr int FL_KV = 4096;
constexpr int FL_O = 32768;
constexpr int FL_H = 65536;
constexpr int LDS_BYTES = 160 * 1024;
constexpr unsigned SPIN_CAP = 1u << 20;
#define WARM_ON 2
#define WARMA 0
#define ROT2 1
#define OUT_AUX 0
#define ROTA 0
#define ROTB 9
#define P2S 4

#define STAMP(i) do {} while (0)
#define STAMP_FLUSH() do {} while (0)

DI float bf2f(u16 v) { return __uint_as_float((unsigned)v << 16); }
DI float bflo(unsigned v) { return __uint_as_float(v << 16); }
DI float bfhi(unsigned v) { return __uint_as_float(v & 0xffff0000u); }
DI unsigned pk2(float lo, float hi) { f32x2 x = {lo, hi}; return __builtin_bit_cast(unsigned, __builtin_convertvector(x, bf16x2)); }
template <int S> DI bf16x8 pack8(const f32x16& x) {
  u32x4 p = {pk2(x[8 * S], x[8 * S + 1]), pk2(x[8 * S + 2], x[8 * S + 3]), pk2(x[8 * S + 4], x[8 * S + 5]), pk2(x[8 * S + 6], x[8 * S + 7])};
  return __builtin_bit_cast(bf16x8, p);
}
template <int N> DI void wait_vm() { asm volatile("s_waitcnt vmcnt(%0)" ::"i"(N) : "memory"); }
DI void wait_lgkm0() { asm volatile("s_waitcnt lgkmcnt(0)" ::: "memory"); }
DI void bar() { __builtin_amdgcn_s_barrier(); }
DI void cfence() { asm volatile("" ::: "memory"); }
DI void sync_lds() { wait_lgkm0(); bar(); cfence(); }
DI bf16x8 lds_b128(LAS char* p) { return *(LAS bf16x8*)p; }
DI s16x4 lds_tr(LAS char* p) { return __builtin_amdgcn_ds_read_tr16_b64_v4i16((LAS s16x4*)p); }
DI bf16x8 cat8(s16x4 lo, s16x4 hi) { return __builtin_bit_cast(bf16x8, __builtin_shufflevector(lo, hi, 0, 1, 2, 3, 4, 5, 6, 7)); }
DI bf16x8 zero8() { s16x8 z = {0, 0, 0, 0, 0, 0, 0, 0}; return __builtin_bit_cast(bf16x8, z); }
DI GAS u64* flagp(char* ws, int off) { return (GAS u64*)(ws + WS_FLAG + off); }
DI float hmax(float v) { auto r = __builtin_amdgcn_permlane32_swap(__float_as_uint(v), __float_as_uint(v), false, false); return fmaxf(__uint_as_float(r[0]), __uint_as_float(r[1])); }
DI float hmin(float v) { auto r = __builtin_amdgcn_permlane32_swap(__float_as_uint(v), __float_as_uint(v), false, false); return fminf(__uint_as_float(r[0]), __uint_as_float(r[1])); }
DI float hsum(float v) { auto r = __builtin_amdgcn_permlane32_swap(__float_as_uint(v), __float_as_uint(v), false, false); return __uint_as_float(r[0]) + __uint_as_float(r[1]); }

#define SCHED() __builtin_amdgcn_sched_barrier(0)
template <int I> struct IC { static constexpr int v = I; };
template <int B, int E, class F> DI void sfor(F f) { if constexpr (B < E) { f(IC<B>{}); sfor<B + 1, E>(f); } }
typedef __amdgpu_buffer_rsrc_t rsrc_t;
DI rsrc_t mk_rsrc(const void* p, int bytes) { return __builtin_amdgcn_make_buffer_rsrc((void*)p, 0, bytes, 0x00020000); }
DI void dma16(rsrc_t r, LAS char* l, unsigned voff, int soff) {
  asm volatile("s_mov_b32 m0, %0\n\ts_nop 0\n\tbuffer_load_dwordx4 %1, %2, %3 offen lds"
               ::"s"((unsigned)(size_t)l), "v"(voff), "s"(r), "s"(soff) : "memory");
}
DI void l2touch(rsrc_t r, unsigned voff, int soff) {
  asm volatile("buffer_load_dword v167, %0, %1, %2 offen" ::"v"(voff), "s"(r), "s"(soff) : "memory", "v167");
}
template <int NM, int NR, int RPM, class M, class R>
DI void mgroup(M mma, R rd) {
  sfor<0, NM>([&](auto i) {
    constexpr int I = decltype(i)::v;
    mma(i); SCHED();
    sfor<0, RPM>([&](auto j) { constexpr int X = I * RPM + decltype(j)::v; if constexpr (X < NR) rd(IC<X>{}); });
    SCHED();
  });
}

template <bool NORM> DI void dual_exp(const f32x16& x, f32x16& pp, f32x16& pm) {
  float mx = fmaxf(x[0], x[1]), mn = fminf(x[0], x[1]);
#pragma unroll
  for (int i = 2; i < 16; i += 2) { mx = fmaxf(fmaxf(mx, x[i]), x[i + 1]); mn = fminf(fminf(mn, x[i]), x[i + 1]); }
  mx = hmax(mx); mn = hmin(mn);
  const float L2E = 1.4426950408889634f;
  const f32x2 l2 = {L2E, L2E}, nmx = {-mx * L2E, -mx * L2E}, pmn = {mn * L2E, mn * L2E};
  f32x2 sp = {0.f, 0.f}, sm = {0.f, 0.f};
#pragma unroll
  for (int i = 0; i < 16; i += 2) {
    const f32x2 xi = {x[i], x[i + 1]};
    const f32x2 a = __builtin_elementwise_fma(xi, l2, nmx), b = __builtin_elementwise_fma(xi, -l2, pmn);
    const f32x2 ea = {__builtin_amdgcn_exp2f(a[0]), __builtin_amdgcn_exp2f(a[1])};
    const f32x2 eb = {__builtin_amdgcn_exp2f(b[0]), __builtin_amdgcn_exp2f(b[1])};
    pp[i] = ea[0]; pp[i + 1] = ea[1]; pm[i] = eb[0]; pm[i + 1] = eb[1];
    if constexpr (NORM) { sp += ea; sm += eb; }
  }
  if constexpr (NORM) {
    const float rp = __builtin_amdgcn_rcpf(hsum(sp[0] + sp[1])), rm = __builtin_amdgcn_rcpf(hsum(sm[0] + sm[1]));
    const f32x2 rp2 = {rp, rp}, rm2 = {rm, rm};
#pragma unroll
    for (int i = 0; i < 16; i += 2) {
      f32x2 a = {pp[i], pp[i + 1]}, b = {pm[i], pm[i + 1]};
      a *= rp2; b *= rm2;
      pp[i] = a[0]; pp[i + 1] = a[1]; pm[i] = b[0]; pm[i + 1] = b[1];
    }
  }
}

DI void poll_flag(GAS u64* p, u64 tag) {
#pragma unroll 1
  for (unsigned spins = 0; spins < SPIN_CAP; ++spins) {
    const u64 v = __hip_atomic_load(p, RLX_AGENT);
    if (__all(v == tag)) break;
    __builtin_amdgcn_s_sleep(1);
  }
}

template <bool IS_A>
DI void phase2_load(LAS char* sm, char* ws, const u16* __restrict__ W0, const u16* __restrict__ W1, int rt2, int ct2, int lw, int lane,
                    int rot, u64 tag, bool upd_epoch) {
  constexpr int NT = IS_A ? 16 : 32;
  constexpr int S = P2S, SB = 32768;
  const int r5 = 8 * lw + (lane >> 3);
  const unsigned voffR = (unsigned)r5 * 2048 + (unsigned)(((lane & 7) ^ ((r5 >> 1) & 7)) * 16);
  const unsigned voffT = (unsigned)r5 * (IS_A ? 2048 : 4096) + (unsigned)(((lane & 7) ^ (((r5 >> 1) & 1) << 2)) * 16);
  const char* gAct = IS_A ? ws + WS_O + (size_t)rt2 * 128 * 64 : ws + WS_H + (size_t)rt2 * 256;
  const int actBytes = IS_A ? 2048 * 2048 - rt2 * 128 * 64 : 2048 * 4096 - rt2 * 256;
  const int oc = (lane & 7) ^ ((r5 >> 1) & 7);
  const unsigned voffO = (unsigned)(oc >> 2) * (2048 * 64) + (unsigned)r5 * 64 + (unsigned)(oc & 3) * 16;
  const char* gW0 = IS_A ? (const char*)W0 + (size_t)ct2 * 256 : (const char*)W0 + (size_t)ct2 * 128 * 2048;
  const char* gW1 = IS_A ? gW0 : (const char*)W1 + (size_t)ct2 * 128 * 2048;
  const int wBytes = IS_A ? 1024 * 2048 - ct2 * 256 : 128 * 2048;
  LAS char* dst0 = sm + lw * 1024;
  auto issAct = [&](int kt, int buf) {
    const rsrc_t r = mk_rsrc(gAct, kt < NT ? actBytes : 0);
    kt = (kt + rot) & (NT - 1);
#pragma unroll
    for (int i = 0; i < 4; ++i) {
      if constexpr (IS_A) dma16(r, dst0 + buf + i * 4096, voffO, (kt * 2 * 2048 + i * 32) * 64);
      else dma16(r, dst0 + buf + i * 4096, voffT, (kt * 64 + (i & 1) * 32) * 4096 + (i >> 1) * 128);
    }
  };
  auto issW = [&](int kt, int buf) {
    const bool live = kt < NT;
    kt = (kt + rot) & (NT - 1);
    const rsrc_t r = mk_rsrc((IS_A || kt < 16) ? gW0 : gW1, live ? wBytes : 0);
#pragma unroll
    for (int i = 0; i < 4; ++i) {
      if constexpr (IS_A) dma16(r, dst0 + buf + 16384 + i * 4096, voffT, (kt * 64 + (i & 1) * 32) * 2048 + (i >> 1) * 128);
      else dma16(r, dst0 + buf + 16384 + i * 4096, voffR, (kt & 15) * 128 + i * 32 * 2048);
    }
  };
#pragma unroll
  for (int s = 0; s < S - 1; ++s) issW(s, s * SB);
  if (lw == 0) {
    poll_flag(flagp(ws, (IS_A ? FL_O : FL_H) + rt2 * 1024 + (lane >> 2) * 64 + (lane & 3) * 8), tag);
    __builtin_amdgcn_fence(__ATOMIC_ACQUIRE, "agent");
    wait_vm<0>();
    if (upd_epoch && lane == 0) __hip_atomic_store(flagp(ws, IS_A ? FL_EPOCH_A : FL_EPOCH_B), tag, RLX_AGENT);
  }
  cfence(); bar(); cfence();
#pragma unroll
  for (int s = 0; s < S - 1; ++s) issAct(s, s * SB);
  int wr = (S - 1) * SB;
#pragma unroll 1
  for (int t = 0; t < NT; ++t) {
    if (t < S - 2) { if (t == 0) wait_vm<4 * (S - 2)>(); else if (t == 1) wait_vm<4 * (S - 2) + 4>(); else wait_vm<4 * (S - 2) + 8>(); }
    else wait_vm<8 * (S - 2)>();
    bar(); cfence();
    issAct(t + S - 1, wr); issW(t + S - 1, wr);
    wr += SB; if (wr == S * SB) wr = 0;
  }
  wait_vm<0>();
}

template <bool IS_A>
DI void phase2(LAS char* sm, char* ws, const u16* __restrict__ Bx, const u16* __restrict__ Dskip, float* __restrict__ out, int rt2, int ct2,
               int wave, int lane, unsigned (&g_st)[2]) {
  char* g_ws = ws;
  constexpr int NT = IS_A ? 16 : 32;
  constexpr int S = P2S, SB = 32768;
  const int h = lane >> 5, kh = wave >> 2, wm = (wave >> 1) & 1, wn = wave & 1;
  const int row = rt2 * 128 + 64 * wm + 32 * kh + (lane & 31);
  const int col0 = ct2 * 128 + 64 * wn + 4 * h;
  u32x2 xr[2][4], dr[2][4];
  if constexpr (!IS_A) {
#pragma unroll
    for (int j = 0; j < 2; ++j)
#pragma unroll
      for (int g = 0; g < 4; ++g) {
        xr[j][g] = *(const u32x2*)(Bx + (size_t)row * 1024 + col0 + 32 * j + 8 * g);
        dr[j][g] = *(const u32x2*)(Dskip + col0 + 32 * j + 8 * g);
      }
  }
  cfence(); bar(); cfence();
  STAMP(19);
  const int xs = (lane >> 1) & 7;
  const int q = (lane & 15) >> 2, p4 = lane & 3, blk = (lane >> 4) & 1;
  int rofs[2], tofs[2];
#pragma unroll
  for (int s = 0; s < 2; ++s) {
    rofs[s] = (IS_A ? 64 * wm * 128 : 16384 + 64 * wn * 128) + (lane & 31) * 128 + 16 * ((2 * (2 * kh + s) + h) ^ xs);
    tofs[s] = (IS_A ? 16384 + wn * 8192 + (32 * kh + 16 * h + q) * 128 : wm * 8192 + (32 * kh + 8 * h + q) * 128) + 32 * blk + 8 * p4 +
              ((64 * s) ^ (64 * ((q >> 1) & 1)));
  }
  constexpr int TS = IS_A ? 512 : 2048, TT = IS_A ? 1024 : 512;
  f32x16 acc[2][2];
#pragma unroll
  for (int i = 0; i < 2; ++i)
#pragma unroll
    for (int j = 0; j < 2; ++j)
#pragma unroll
      for (int e = 0; e < 16; ++e) acc[i][j][e] = 0.f;
  {
    bf16x8 fr[2][2];
    s16x4 tl[2][2][2];
#pragma unroll
    for (int i = 0; i < 2; ++i) { fr[1][i] = zero8(); tl[1][i][0] = (s16x4){0, 0, 0, 0}; tl[1][i][1] = (s16x4){0, 0, 0, 0}; }
    auto mma = [&](auto B_, auto I) {
      constexpr int B = decltype(B_)::v, i = decltype(I)::v, ri = i >> 1, ti = i & 1;
      if constexpr (IS_A) acc[ri][ti] = MFMA32(cat8(tl[B][ti][0], tl[B][ti][1]), fr[B][ri], acc[ri][ti]);
      else acc[ti][ri] = MFMA32(fr[B][ri], cat8(tl[B][ti][0], tl[B][ti][1]), acc[ti][ri]);
    };
    auto rdf = [&](auto B_, auto S_, auto X, LAS char* sb) {
      constexpr int B = decltype(B_)::v, s = decltype(S_)::v, x = decltype(X)::v;
      if constexpr (x == 0) fr[B][0] = lds_b128(sb + rofs[s]);
      else if constexpr (x < 5) tl[B][(x - 1) >> 1][(x - 1) & 1] = lds_tr(sb + tofs[(x - 1) >> 1] + s * TS + ((x - 1) & 1) * TT);
      else fr[B][1] = lds_b128(sb + rofs[s] + 4096);
    };
    int rd = 0;
#pragma unroll 1
    for (int t = 0; t < NT; ++t) {
      wait_lgkm0(); bar(); cfence(); SCHED();
      LAS char* sb = sm + rd;
      mgroup<4, 6, 2>([&](auto i) { mma(IC<1>{}, i); }, [&](auto x) { rdf(IC<0>{}, IC<0>{}, x, sb); });
      mgroup<4, 6, 2>([&](auto i) { mma(IC<0>{}, i); }, [&](auto x) { rdf(IC<1>{}, IC<1>{}, x, sb); });
      rd += SB; if (rd == S * SB) rd = 0;
    }
    sfor<0, 4>([&](auto i) { mma(IC<1>{}, i); });
  }
  STAMP(20);
  sync_lds();
  {
    LAS char* mine = sm + wave * 8192 + lane * 16;
#pragma unroll
    for (int j = 0; j < 2; ++j)
#pragma unroll
      for (int g = 0; g < 4; ++g) {
        f32x4 v;
#pragma unroll
        for (int e = 0; e < 4; ++e) v[e] = kh ? acc[0][j][4 * g + e] : acc[1][j][4 * g + e];
        *(LAS f32x4*)(mine + (j * 4 + g) * 1024) = v;
      }
  }
  sync_lds();
  {
    LAS char* theirs = sm + (wave ^ 4) * 8192 + lane * 16;
    const rsrc_t ors = mk_rsrc(out, 2048 * 2048 * 4);
    const int obase = (row * 2048 + (IS_A ? 0 : 1024) + col0) * 4;
#pragma unroll
    for (int j = 0; j < 2; ++j)
#pragma unroll
      for (int g = 0; g < 4; ++g) {
        f32x4 v = *(LAS f32x4*)(theirs + (j * 4 + g) * 1024);
#pragma unroll
        for (int e = 0; e < 4; ++e) v[e] += kh ? acc[1][j][4 * g + e] : acc[0][j][4 * g + e];
        if constexpr (!IS_A) {
          v[0] = fmaf(bflo(xr[j][g][0]), bflo(dr[j][g][0]), v[0]); v[1] = fmaf(bfhi(xr[j][g][0]), bfhi(dr[j][g][0]), v[1]);
          v[2] = fmaf(bflo(xr[j][g][1]), bflo(dr[j][g][1]), v[2]); v[3] = fmaf(bfhi(xr[j][g][1]), bfhi(dr[j][g][1]), v[3]);
        }
        __builtin_amdgcn_raw_buffer_store_b128(__builtin_bit_cast(u32x4, v), ors, obase + 128 * j + 32 * g, 0, OUT_AUX);
      }
  }
  STAMP(21);
}

__global__ __launch_bounds__(768, 3) void fused_kernel(
    const u16* __restrict__ Ax, const u16* __restrict__ Wqkv, const u16* __restrict__ Wphi, const u16* __restrict__ Wo,
    const u16* __restrict__ Bx, const u16* __restrict__ Lre, const u16* __restrict__ Lim, const u16* __restrict__ Bre,
    const u16* __restrict__ Bim, const u16* __restrict__ Cre, const u16* __restrict__ Cim, const u16* __restrict__ Logdt,
    const u16* __restrict__ Dskip, float* __restrict__ out, char* ws) {
  __shared__ __attribute__((aligned(1024))) char smem[LDS_BYTES];
  LAS char* sm = (LAS char*)smem;
  char* g_ws = ws;
  unsigned g_st[2] = {0u, 0u};
  STAMP(0);
  const int tid = threadIdx.x, lane = tid & 63, wave = __builtin_amdgcn_readfirstlane(tid >> 6);
  const int b = blockIdx.x, xcd = b & 7, idx = b >> 3, i16 = idx >> 1;
  const bool isA = (idx & 1) == 0;
  const int rt = 4 * (xcd & 1) + (i16 & 3), cg = 4 * (xcd >> 1) + (i16 >> 2);
  const int rt2 = 4 * (xcd & 3) + (i16 & 3), ct2 = 4 * (xcd >> 2) + (i16 >> 2);
  const int h = lane >> 5, c32 = lane & 31;
  const int rotx = 4 * (xcd >> 1) + 8 * (xcd & 1);
  const int rot1 = (isA ? ROTA : ROTB) == 9 ? rotx : (isA ? ROTA : ROTB) * xcd;
  constexpr int WR_OFF = 98304;
  constexpr int PH_PITCH = 136, PH_BYTES = 64 * PH_PITCH;
  constexpr int RED_OFF = 8 * PH_BYTES;
  constexpr int RED_W = 8192 + 256;
  constexpr int RED_GAP = 6912;
  constexpr int KVF_OFF = 147456;
  constexpr int SC_OFF = 98304;
  constexpr int SB1 = 49152;
  constexpr int TE_OFF = 3 * SB1;
  constexpr int TG_OFF = TE_OFF + 4096;
  constexpr int TC_OFF = TG_OFF + 4096;

  if (wave >= 8) {
    const int lw = wave - 8;
    const int r5 = 8 * lw + (lane >> 3);
    const unsigned rowchunk = (unsigned)(((lane & 7) ^ ((r5 >> 1) & 7)) * 16);
    LAS char* dst0 = sm + lw * 1024;
    if (isA) {
      const unsigned voffA = (unsigned)r5 * 2048 + rowchunk;
      const unsigned voffB = (unsigned)r5 * 6144 + (unsigned)(((lane & 7) ^ (((r5 >> 1) & 1) << 2)) * 16);
      const char* gA = (const char*)Ax + (size_t)rt * 256 * 2048;
      const char* gB = (const char*)Wqkv + (size_t)cg * 128;
      auto issX = [&](int kt, int buf) {
        const rsrc_t r = mk_rsrc(gA, kt < 16 ? 256 * 2048 : 0);
#pragma unroll
        for (int i = 0; i < 8; ++i) dma16(r, dst0 + buf + i * 4096, voffA, ((kt + rot1) & 15) * 128 + i * 32 * 2048);
      };
      auto issW = [&](int j, int kt, int u) {
        const rsrc_t r = mk_rsrc(gB, kt < 16 ? 1024 * 6144 - cg * 128 : 0);
#pragma unroll
        for (int i = 0; i < 2; ++i) dma16(r, dst0 + WR_OFF + (u & 7) * 8192 + i * 4096, voffB, (((kt + rot1) & 15) * 64 + i * 32) * 6144 + j * 2048);
      };
      issX(0, 0); issW(0, 0, 0); issW(1, 0, 1); issW(2, 0, 2);
      u64 epoch = __hip_atomic_load(flagp(ws, FL_EPOCH_A), RLX_AGENT);
      u16 wraw[8];
#pragma unroll
      for (int j = 0; j < 8; ++j) wraw[j] = Wphi[(size_t)(2 * cg + (lw >> 1)) * 1024 + (16 * (lw & 1) + 8 * (j >> 2) + 4 * h + (j & 3)) * 32 + c32];
      cfence();
      issX(1, 32768); issW(0, 1, 3); issW(1, 1, 4);
      int xw = 65536, u0 = 0;
#pragma unroll 1
      for (int t = 0; t < 16; ++t) {
        wait_vm<12>(); bar(); cfence();
        issW(2, t + 1, u0 + 5); issX(t + 2, xw); issW(0, t + 2, u0 + 6); issW(1, t + 2, u0 + 7);
        xw += 32768; if (xw == 98304) xw = 0;
        u0 = (u0 + 3) & 7;
      }
      wait_vm<0>();
      {
        s16x8 w8;
#pragma unroll
        for (int j = 0; j < 8; ++j) w8[j] = (short)wraw[j];
        *(LAS s16x8*)(sm + WR_OFF + 2 * 8192 + lw * 1024 + lane * 16) = w8;
      }
      const u64 tag = epoch + 1ull;
      sync_lds();
      __builtin_amdgcn_s_setprio(3);
      bar(); cfence();
      {
        const rsrc_t kvrs = mk_rsrc(ws + WS_KVP + (size_t)(cg * 8 + rt) * KVP_BYTES, KVP_BYTES);
#pragma unroll
        for (int j = 0; j < 3; ++j) {
          if (j < 2 || lw == 0) {
            const int zl = lane & 15, zh = zl >> 3, zks = (zl >> 1) & 3, zhh = zl & 1;
            f32x4 s0 = {0.f, 0.f, 0.f, 0.f}, s1 = {0.f, 0.f, 0.f, 0.f};
#pragma unroll
            for (int w = 0; w < 4; ++w) {
              if (j < 2) {
                LAS char* rp = sm + RED_OFF + (2 * w + j) * RED_W + (2 * w + j >= 5 ? RED_GAP : 0) + (2 * lw) * 1024 + lane * 16;
                s0 += *(LAS f32x4*)rp; s1 += *(LAS f32x4*)(rp + 1024);
              } else {
                LAS char* rp = sm + RED_OFF + (2 * w + zh) * RED_W + (2 * w + zh >= 5 ? RED_GAP : 0) + 8192 + (((zks >> 1) * 2 + zhh) * 4 + 2 * (zks & 1)) * 16;
                s0 += *(LAS f32x4*)rp; s1 += *(LAS f32x4*)(rp + 16);
              }
            }
            const u32x4 d = {pk2(s0[0], s0[1]), pk2(s0[2], s0[3]), pk2(s1[0], s1[1]), pk2(s1[2], s1[3])};
            if (j < 2) __builtin_amdgcn_raw_buffer_store_b128(d, kvrs, ((j * 4 + lw) * 64 + lane) * 16, 0, 16);
            else if (lane < 16) __builtin_amdgcn_raw_buffer_store_b128(d, kvrs, 8192 + zl * 16, 0, 16);
          }
        }
        wait_vm<0>();
        if (lane == 0) __hip_atomic_store(flagp(ws, FL_KV + (cg * 8 + rt) * 128 + lw * 8), tag, RLX_AGENT);
        if (lw == 0) poll_flag(flagp(ws, FL_KV + (cg * 8 + ((lane >> 2) & 7)) * 128 + (lane & 3) * 8), tag);
      }
      __builtin_amdgcn_s_setprio(0);
      cfence(); bar();
      bar(); cfence();
      phase2_load<true>(sm, ws, Wo, Wo, rt2, ct2, lw, lane, ROT2 * (4 * (xcd & 3) + 8 * (xcd >> 2)), tag, b == 0);
    } else {
      const int tloc = 32 * ((r5 >> 2) & 1) + 16 * (r5 & 1) + ((r5 & 3) >> 1) + 2 * (r5 >> 3);
      const unsigned voffA = (unsigned)tloc * 2048 + rowchunk;
      const unsigned voffB = (unsigned)r5 * 2048 + rowchunk;
      const char* gA = (const char*)Bx + (size_t)rt * 256 * 2048;
      const char* gBr = (const char*)Bre + (size_t)cg * 64 * 2048;
      const char* gBi = (const char*)Bim + (size_t)cg * 64 * 2048;
      auto issue = [&](int kt, int buf) {
        const bool live = kt < 16;
        kt = (kt + rot1) & 15;
        const rsrc_t ra = mk_rsrc(gA, live ? 256 * 2048 : 0), rr = mk_rsrc(gBr, live ? 64 * 2048 : 0), ri = mk_rsrc(gBi, live ? 64 * 2048 : 0);
#pragma unroll
        for (int i = 0; i < 8; ++i) dma16(ra, dst0 + buf + i * 4096, voffA, kt * 128 + ((i >> 1) * 64 + (i & 1) * 8) * 2048);
#pragma unroll
        for (int i = 8; i < 12; ++i) dma16(i < 10 ? rr : ri, dst0 + buf + i * 4096, voffB, kt * 128 + (i & 1) * 32 * 2048);
      };
      issue(0, 0);
      u64 epoch = __hip_atomic_load(flagp(ws, FL_EPOCH_B), RLX_AGENT);
      cfence();
      issue(1, SB1);
      int wr = 2 * SB1;
#pragma unroll 1
      for (int t = 0; t < 16; ++t) {
        wait_vm<12>(); bar(); cfence();
        issue(t + 2, wr);
        wr += SB1; if (wr == 3 * SB1) wr = 0;
      }
      wait_vm<0>();
      const u64 tag = epoch + 1ull;
      const unsigned tlo = (unsigned)tag, thi = (unsigned)(tag >> 32);
      bar();
      {
        const rsrc_t agrs = mk_rsrc(ws + WS_AGG + (size_t)cg * 8192, 8192);
        u32x4 gr[2];
#pragma unroll 1
        for (unsigned spins = 0; spins < SPIN_CAP; ++spins) {
          unsigned bad = 0;
#pragma unroll
          for (int k = 0; k < 2; ++k) gr[k] = __builtin_amdgcn_raw_buffer_load_b128(agrs, ((lw + 4 * k) * 64 + lane) * 16, 0, 16);
#pragma unroll
          for (int k = 0; k < 2; ++k) if (lw + 4 * k < rt) bad |= (gr[k][1] ^ tlo) | (gr[k][3] ^ thi);
          if (__all(bad == 0)) break;
          __builtin_amdgcn_s_sleep(1);
        }
#pragma unroll
        for (int k = 0; k < 2; ++k)
          *(LAS f32x2*)(sm + TG_OFF + ((lw + 4 * k) * 64 + lane) * 8) = (f32x2){__uint_as_float(gr[k][0]), __uint_as_float(gr[k][2])};
      }
      sync_lds();
      phase2_load<false>(sm, ws, Cre, Cim, rt2, ct2, lw, lane, ROT2 * (8 * (xcd & 3) + 16 * (xcd >> 2)), tag, rt2 == 15 && ct2 == 0);
    }
    return;
  }

  const int wm = wave >> 1, wn = wave & 1;
  const int xs = (lane >> 1) & 7;
  const int q = (lane & 15) >> 2, p4 = lane & 3, blk = (lane >> 4) & 1;
  int rofs[4];
#pragma unroll
  for (int s = 0; s < 4; ++s) rofs[s] = c32 * 128 + 16 * ((2 * s + h) ^ xs);

  if (isA) {
    u64 epoch = __hip_atomic_load(flagp(ws, FL_EPOCH_A), RLX_AGENT);
    if (WARMA) {
      const int sl = (xcd * 16 + i16) * 8 + wave;
      const uintptr_t p0 = (uintptr_t)Wqkv, p1 = (uintptr_t)Ax;
      l2touch(mk_rsrc((const void*)(sl < 768 ? p0 : p1), (sl < 768 || WARMA > 1) ? 0x7fffffff : 0), (unsigned)lane * 128, (sl < 768 ? sl : 2 * (sl - 768)) * 8192);
      if (WARMA > 2) l2touch(mk_rsrc((const void*)p1, sl >= 768 ? 0x7fffffff : 0), (unsigned)lane * 128, (2 * (sl - 768) + 1) * 8192);
      wait_vm<0>();
    }
    const int tofs = WR_OFF + (8 * h + q) * 128 + 64 * (wn ^ ((q >> 1) & 1)) + 32 * blk + 8 * p4;
    f32x16 Qt[2], Kt[2], V[2];
#pragma unroll
    for (int m = 0; m < 2; ++m)
#pragma unroll
      for (int e = 0; e < 16; ++e) { Qt[m][e] = 0.f; Kt[m][e] = 0.f; V[m][e] = 0.f; }
    STAMP(1);
    {
      bf16x8 fx[2][2];
      s16x4 fw[2][3][2];
#pragma unroll
      for (int i = 0; i < 2; ++i) fx[1][i] = zero8();
#pragma unroll
      for (int j = 0; j < 3; ++j) { fw[1][j][0] = (s16x4){0, 0, 0, 0}; fw[1][j][1] = (s16x4){0, 0, 0, 0}; }
      auto mma = [&](auto B_, auto I) {
        constexpr int B = decltype(B_)::v, i = decltype(I)::v, m = i / 3, j = i % 3;
        const bf16x8 w = cat8(fw[B][j][0], fw[B][j][1]);
        if constexpr (j == 0) Qt[m] = MFMA32(w, fx[B][m], Qt[m]);
        else if constexpr (j == 1) Kt[m] = MFMA32(w, fx[B][m], Kt[m]);
        else V[m] = MFMA32(fx[B][m], w, V[m]);
      };
      int xr = 0, u0 = 0;
#pragma unroll 1
      for (int t = 0; t < 16; ++t) {
        wait_lgkm0(); bar(); cfence(); SCHED();
        LAS char* xb = sm + xr + wm * 8192;
        LAS char* wb[3] = {sm + tofs + (u0 & 7) * 8192, sm + tofs + ((u0 + 1) & 7) * 8192, sm + tofs + ((u0 + 2) & 7) * 8192};
        auto rdf = [&](auto B_, auto S_, auto X) {
          constexpr int B = decltype(B_)::v, s = decltype(S_)::v, x = decltype(X)::v;
          if constexpr (x == 0) fx[B][0] = lds_b128(xb + rofs[s]);
          else if constexpr (x < 7) fw[B][(x - 1) >> 1][(x - 1) & 1] = lds_tr(wb[(x - 1) >> 1] + s * 2048 + ((x - 1) & 1) * 512);
          else fx[B][1] = lds_b128(xb + rofs[s] + 4096);
        };
        mgroup<6, 8, 2>([&](auto i) { mma(IC<1>{}, i); }, [&](auto x) { rdf(IC<0>{}, IC<0>{}, x); });
        mgroup<6, 8, 2>([&](auto i) { mma(IC<0>{}, i); }, [&](auto x) { rdf(IC<1>{}, IC<1>{}, x); });
        mgroup<6, 8, 2>([&](auto i) { mma(IC<1>{}, i); }, [&](auto x) { rdf(IC<0>{}, IC<2>{}, x); });
        mgroup<6, 8, 2>([&](auto i) { mma(IC<0>{}, i); }, [&](auto x) { rdf(IC<1>{}, IC<3>{}, x); });
        xr += 32768; if (xr == 98304) xr = 0;
        u0 = (u0 + 3) & 7;
      }
      sfor<0, 6>([&](auto i) { mma(IC<1>{}, i); });
    }
    asm volatile("" : "+v"(epoch));
    const u64 tag = epoch + 1ull;
    STAMP(2);
    sync_lds();
    STAMP(3);
    const bf16x8 wf0 = lds_b128(sm + WR_OFF + 2 * 8192 + (2 * wn) * 1024 + lane * 16);
    const bf16x8 wf1 = lds_b128(sm + WR_OFF + 2 * 8192 + (2 * wn + 1) * 1024 + lane * 16);
    f32x16 zero16;
#pragma unroll
    for (int e = 0; e < 16; ++e) zero16[e] = 0.f;
    LAS char* ph = sm + wave * PH_BYTES;
#pragma unroll
    for (int m = 0; m < 2; ++m) {
      f32x16 pk = MFMA32(wf0, pack8<0>(Kt[m]), zero16);
      pk = MFMA32(wf1, pack8<1>(Kt[m]), pk);
      f32x16 pp, pm;
      dual_exp<true>(pk, pp, pm);
      LAS char* rowp = ph + (32 * m + c32) * PH_PITCH + 8 * h;
#pragma unroll
      for (int g = 0; g < 4; ++g) {
        u32x2 a = {pk2(pp[4 * g], pp[4 * g + 1]), pk2(pp[4 * g + 2], pp[4 * g + 3])};
        u32x2 c = {pk2(pm[4 * g], pm[4 * g + 1]), pk2(pm[4 * g + 2], pm[4 * g + 3])};
        *(LAS u32x2*)(rowp + 16 * g) = a;
        *(LAS u32x2*)(rowp + 64 + 16 * g) = c;
      }
    }
    wait_lgkm0();
    STAMP(4);
    f32x16 KV[2], Z[2];
    KV[0] = zero16; KV[1] = zero16; Z[0] = zero16; Z[1] = zero16;
    {
      s16x8 o8;
#pragma unroll
      for (int j = 0; j < 8; ++j) o8[j] = (short)0x3f80;
      const bf16x8 ones = __builtin_bit_cast(bf16x8, o8);
      LAS char* tp = ph + (4 * h + q) * PH_PITCH + 32 * blk + 8 * p4;
#pragma unroll
      for (int m = 0; m < 2; ++m) {
#pragma unroll
        for (int s = 0; s < 2; ++s) {
          const bf16x8 vb = s ? pack8<1>(V[m]) : pack8<0>(V[m]);
          LAS char* t0 = tp + (32 * m + 16 * s) * PH_PITCH;
#pragma unroll
          for (int fs = 0; fs < 2; ++fs) {
            const bf16x8 af = cat8(lds_tr(t0 + 64 * fs), lds_tr(t0 + 8 * PH_PITCH + 64 * fs));
            KV[fs] = MFMA32(af, vb, KV[fs]);
            Z[fs] = MFMA32(af, ones, Z[fs]);
          }
        }
      }
    }
    {
      LAS char* rw = sm + RED_OFF + wave * RED_W + (wave >= 5 ? RED_GAP : 0);
#pragma unroll
      for (int fs = 0; fs < 2; ++fs)
#pragma unroll
        for (int g = 0; g < 4; ++g) {
          f32x4 v = {KV[fs][4 * g], KV[fs][4 * g + 1], KV[fs][4 * g + 2], KV[fs][4 * g + 3]};
          *(LAS f32x4*)(rw + (fs * 4 + g) * 1024 + lane * 16) = v;
        }
      if (c32 == 0) {
#pragma unroll
        for (int fs = 0; fs < 2; ++fs)
#pragma unroll
          for (int g = 0; g < 4; ++g) {
            f32x4 z = {Z[fs][4 * g], Z[fs][4 * g + 1], Z[fs][4 * g + 2], Z[fs][4 * g + 3]};
            *(LAS f32x4*)(rw + 8192 + ((fs * 2 + h) * 4 + g) * 16) = z;
          }
      }
    }
    STAMP(5);
    sync_lds();
    STAMP(6);
    STAMP(9);
    bf16x8 fq[2][4];
#pragma unroll
    for (int m = 0; m < 2; ++m) {
      f32x16 pq = MFMA32(wf0, pack8<0>(Qt[m]), zero16);
      pq = MFMA32(wf1, pack8<1>(Qt[m]), pq);
      f32x16 pp, pm;
      dual_exp<false>(pq, pp, pm);
      fq[m][0] = pack8<0>(pp); fq[m][1] = pack8<1>(pp); fq[m][2] = pack8<0>(pm); fq[m][3] = pack8<1>(pm);
    }
    asm volatile("" : "+v"(fq[0][0]), "+v"(fq[0][1]), "+v"(fq[0][2]), "+v"(fq[0][3]), "+v"(fq[1][0]), "+v"(fq[1][1]), "+v"(fq[1][2]), "+v"(fq[1][3]));
    STAMP(10);
    cfence(); bar(); cfence();
    STAMP(12);
    {
      const rsrc_t kvrs = mk_rsrc(ws + WS_KVP + (size_t)cg * 8 * KVP_BYTES, 8 * KVP_BYTES);
      u32x4 v[8], vz[8];
#pragma unroll
      for (int r = 0; r < 8; ++r) v[r] = __builtin_amdgcn_raw_buffer_load_b128(kvrs, tid * 16, r * KVP_BYTES, 16);
      if (wave == 7) {
#pragma unroll
        for (int r = 0; r < 8; ++r) vz[r] = __builtin_amdgcn_raw_buffer_load_b128(kvrs, 8192 + (lane & 15) * 16, r * KVP_BYTES, 16);
      }
      auto sum8 = [&](const u32x4 (&x)[8]) {
        f32x4 a = {0.f, 0.f, 0.f, 0.f}, c = {0.f, 0.f, 0.f, 0.f};
#pragma unroll
        for (int r = 0; r < 8; ++r) {
          a += (f32x4){bflo(x[r][0]), bfhi(x[r][0]), bflo(x[r][1]), bfhi(x[r][1])};
          c += (f32x4){bflo(x[r][2]), bfhi(x[r][2]), bflo(x[r][3]), bfhi(x[r][3])};
        }
        return (u32x4){pk2(a[0], a[1]), pk2(a[2], a[3]), pk2(c[0], c[1]), pk2(c[2], c[3])};
      };
      *(LAS u32x4*)(sm + KVF_OFF + tid * 16) = sum8(v);
      if (wave == 7) { const u32x4 z = sum8(vz); if (lane < 16) *(LAS u32x4*)(sm + KVF_OFF + 8192 + lane * 16) = z; }
    }
    STAMP(13);
    sync_lds();
    STAMP(14);
    {
      bf16x8 kf[4], zf[4];
      s16x8 o8;
#pragma unroll
      for (int j = 0; j < 8; ++j) o8[j] = (short)0x3f80;
#pragma unroll
      for (int ks = 0; ks < 4; ++ks) {
        kf[ks] = lds_b128(sm + KVF_OFF + wn * 4096 + ks * 1024 + lane * 16);
        const bf16x8 z = lds_b128(sm + KVF_OFF + 8192 + wn * 128 + (ks * 2 + h) * 16);
        zf[ks] = (lane & 1) ? __builtin_bit_cast(bf16x8, o8) : z;
      }
      LAS char* sc = sm + SC_OFF + wave * 5120;
#pragma unroll
      for (int m = 0; m < 2; ++m) {
        f32x16 np = MFMA32(kf[0], fq[m][0], zero16), nm = MFMA32(kf[2], fq[m][2], zero16);
        f32x16 dp = MFMA32(zf[0], fq[m][0], zero16), dm = MFMA32(zf[2], fq[m][2], zero16);
        np = MFMA32(kf[1], fq[m][1], np); nm = MFMA32(kf[3], fq[m][3], nm);
        dp = MFMA32(zf[1], fq[m][1], dp); dm = MFMA32(zf[3], fq[m][3], dm);
        const float rp = __builtin_amdgcn_rcpf(dp[1]), rm = __builtin_amdgcn_rcpf(dm[1]);
        const float rinv = 1.0f / (dp[0] * rp + dm[0] * rm + 1e-6f);
        const float cp = rp * rinv, cm = rm * rinv;
        f32x16 o;
#pragma unroll
        for (int e = 0; e < 16; ++e) o[e] = np[e] * cp + nm[e] * cm;
        LAS char* op = sc + (32 * m + c32) * 80 + h * 32;
        *(LAS bf16x8*)op = pack8<0>(o);
        *(LAS bf16x8*)(op + 16) = pack8<1>(o);
      }
      wait_lgkm0();
      const rsrc_t ors = mk_rsrc(ws + WS_O, 2048 * 2048);
      const int obase = ((2 * cg + wn) * 2048 + rt * 256 + 64 * wm) * 64 + lane * 16;
#pragma unroll
      for (int j = 0; j < 4; ++j) {
        const u32x4 v = *(LAS u32x4*)(sc + (16 * j + (lane >> 2)) * 80 + (lane & 3) * 16);
        __builtin_amdgcn_raw_buffer_store_b128(v, ors, obase + j * 1024, 0, 16);
      }
    }
    STAMP(15);
    wait_vm<0>();
    STAMP(16);
    if (lane == 0) __hip_atomic_store(flagp(ws, FL_O + ((2 * rt + (wave >> 2)) * 16 + cg) * 64 + (wave & 3) * 8), tag, RLX_AGENT);
    phase2<true>(sm, ws, Bx, Dskip, out, rt2, ct2, wave, lane, g_st);
  } else {
    u64 epoch = __hip_atomic_load(flagp(ws, FL_EPOCH_B), RLX_AGENT);
    const uintptr_t pw0 = (uintptr_t)Wqkv, pw1 = (uintptr_t)Cre, pw2 = (uintptr_t)Cim, pw3 = (uintptr_t)Wo;
    if (wave == 7) {
      const int ch = 64 * cg + lane;
      const float lamr = bf2f(Lre[ch]), lami = bf2f(Lim[ch]);
      const float dt = expf(bf2f(Logdt[ch]));
      const float zr = dt * lamr, zi = dt * lami;
      float sn, cs;
      sincosf(zi, &sn, &cs);
      const float mag = expf(zr);
      const float lr = mag * cs, li = mag * sn;
      const float nr = lr - 1.0f, ni = li, dd = 2.0f / (lamr * lamr + lami * lami);
      const float c2r = (nr * lamr + ni * lami) * dd, c2i = (ni * lamr - nr * lami) * dd;
      float pr = lr, pi = li;
#pragma unroll
      for (int i = 0; i < 4; ++i) { const float t = pr * pr - pi * pi; pi = 2.0f * pr * pi; pr = t; }
      const float l16r = pr, l16i = pi;
      { const float t = pr * pr - pi * pi; pi = 2.0f * pr * pi; pr = t; }
      const float l32r = pr, l32i = pi;
#pragma unroll
      for (int i = 0; i < 3; ++i) { const float t = pr * pr - pi * pi; pi = 2.0f * pr * pi; pr = t; }
      LAS f32x4* tc = (LAS f32x4*)(sm + TC_OFF + lane * 48);
      tc[0] = (f32x4){lr, li, c2r, c2i}; tc[1] = (f32x4){l16r, l16i, l32r, l32i}; tc[2] = (f32x4){pr, pi, 0.f, 0.f};
    }
    f32x16 Ur[2], Ui[2];
#pragma unroll
    for (int m = 0; m < 2; ++m)
#pragma unroll
      for (int e = 0; e < 16; ++e) { Ur[m][e] = 0.f; Ui[m][e] = 0.f; }
    STAMP(1);
    {
      bf16x8 fx[2][2], fb[2][2];
#pragma unroll
      for (int i = 0; i < 2; ++i) { fx[1][i] = zero8(); fb[1][i] = zero8(); }
      auto mma = [&](auto B_, auto I) {
        constexpr int B = decltype(B_)::v, i = decltype(I)::v;
        if constexpr (i == 0) Ur[0] = MFMA32(fx[B][0], fb[B][0], Ur[0]);
        else if constexpr (i == 1) Ui[0] = MFMA32(fx[B][0], fb[B][1], Ui[0]);
        else if constexpr (i == 2) Ur[1] = MFMA32(fx[B][1], fb[B][0], Ur[1]);
        else Ui[1] = MFMA32(fx[B][1], fb[B][1], Ui[1]);
      };
      int rd = 0;
#pragma unroll 1
      for (int t = 0; t < 16; ++t) {
        wait_lgkm0(); bar(); cfence(); SCHED();
        if (WARM_ON && (t & 7) == wave && t < 12 && (t >= 6 || WARM_ON > 1)) {
          const uintptr_t wb_ = t < 6 ? pw0 : (t < 8 ? pw1 : (t < 10 ? pw2 : pw3));
          l2touch(mk_rsrc((const void*)wb_, 0x7fffffff), (unsigned)lane * 128, ((t < 6 ? t : (t & 1)) * 128 + xcd * 16 + i16) * 8192);
        }
        LAS char* xb = sm + rd + wm * 8192;
        LAS char* bb = sm + rd + 32768 + wn * 4096;
        auto rdf = [&](auto B_, auto S_, auto X) {
          constexpr int B = decltype(B_)::v, s = decltype(S_)::v, x = decltype(X)::v;
          if constexpr (x == 0) fx[B][0] = lds_b128(xb + rofs[s]);
          else if constexpr (x == 1) fb[B][0] = lds_b128(bb + rofs[s]);
          else if constexpr (x == 2) fb[B][1] = lds_b128(bb + rofs[s] + 8192);
          else fx[B][1] = lds_b128(xb + rofs[s] + 4096);
        };
        mgroup<4, 4, 2>([&](auto i) { mma(IC<1>{}, i); }, [&](auto x) { rdf(IC<0>{}, IC<0>{}, x); });
        mgroup<4, 4, 2>([&](auto i) { mma(IC<0>{}, i); }, [&](auto x) { rdf(IC<1>{}, IC<1>{}, x); });
        mgroup<4, 4, 2>([&](auto i) { mma(IC<1>{}, i); }, [&](auto x) { rdf(IC<0>{}, IC<2>{}, x); });
        mgroup<4, 4, 2>([&](auto i) { mma(IC<0>{}, i); }, [&](auto x) { rdf(IC<1>{}, IC<3>{}, x); });
        rd += SB1; if (rd == 3 * SB1) rd = 0;
      }
      sfor<0, 4>([&](auto i) { mma(IC<1>{}, i); });
    }
    asm volatile("" : "+v"(epoch));
    const u64 tag = epoch + 1ull;
    const unsigned tlo = (unsigned)tag, thi = (unsigned)(tag >> 32);
    STAMP(2);
    const int seg = 2 * wm + h, chl = 32 * wn + c32;
    const f32x4 k0 = *(LAS f32x4*)(sm + TC_OFF + chl * 48), k1 = *(LAS f32x4*)(sm + TC_OFF + chl * 48 + 16), k2 = *(LAS f32x4*)(sm + TC_OFF + chl * 48 + 32);
    const f32x2 lr2 = {k0[0], k0[0]}, li2 = {k0[1], k0[1]}, cr2 = {k0[2], k0[2]}, ci2 = {k0[3], k0[3]};
    const float l16r = k1[0], l16i = k1[1], l32r = k1[2], l32i = k1[3], l256r = k2[0], l256i = k2[1];
    f32x2 er = {0.f, 0.f}, ei = {0.f, 0.f};
#pragma unroll
    for (int m = 0; m < 2; ++m)
#pragma unroll
      for (int i = 0; i < 16; i += 2) {
        const f32x2 ur = {Ur[m][i], Ur[m][i + 1]}, ui = {Ui[m][i], Ui[m][i + 1]};
        const f32x2 nr = __builtin_elementwise_fma(lr2, er, __builtin_elementwise_fma(-li2, ei, ur));
        const f32x2 ni = __builtin_elementwise_fma(lr2, ei, __builtin_elementwise_fma(li2, er, ui));
        er = nr; ei = ni;
      }
    const float e0r = er[0], e0i = ei[0];
    *(LAS f32x2*)(sm + TE_OFF + (seg * 64 + chl) * 8) =
        (f32x2){fmaf(l16r, e0r, fmaf(-l16i, e0i, er[1])), fmaf(l16r, e0i, fmaf(l16i, e0r, ei[1]))};
    sync_lds();
    f32x2 E[8];
#pragma unroll
    for (int s = 0; s < 8; ++s) E[s] = *(LAS f32x2*)(sm + TE_OFF + (s * 64 + chl) * 8);
    const rsrc_t agrs = mk_rsrc(ws + WS_AGG + (size_t)cg * 8192, 8192);
    if (wm == 0) {
      float tr = 0.f, ti = 0.f;
#pragma unroll
      for (int s = 0; s < 8; ++s) { const float nr = fmaf(l32r, tr, fmaf(-l32i, ti, E[s][0])), ni = fmaf(l32r, ti, fmaf(l32i, tr, E[s][1])); tr = nr; ti = ni; }
      if (h == 0) {
        const u32x4 gr = {__float_as_uint(tr), tlo, __float_as_uint(ti), thi};
        __builtin_amdgcn_raw_buffer_store_b128(gr, agrs, (rt * 64 + chl) * 16, 0, 16);
      }
    }
    STAMP(4);
    sync_lds();
    float sr = 0.f, si = 0.f;
#pragma unroll 1
    for (int r = 0; r < rt; ++r) {
      const f32x2 g = *(LAS f32x2*)(sm + TG_OFF + (r * 64 + chl) * 8);
      const float nr = fmaf(l256r, sr, fmaf(-l256i, si, g[0])), ni = fmaf(l256r, si, fmaf(l256i, sr, g[1]));
      sr = nr; si = ni;
    }
#pragma unroll
    for (int s = 0; s < 7; ++s) {
      const float nr = fmaf(l32r, sr, fmaf(-l32i, si, E[s][0])), ni = fmaf(l32r, si, fmaf(l32i, sr, E[s][1]));
      if (s < seg) { sr = nr; si = ni; }
    }
    er = (f32x2){sr, fmaf(l16r, sr, fmaf(-l16i, si, e0r))};
    ei = (f32x2){si, fmaf(l16r, si, fmaf(l16i, sr, e0i))};
    u32x4 wre[2][2], wim[2][2];
#pragma unroll
    for (int m = 0; m < 2; ++m) {
      f32x2 hr[8], hi[8];
#pragma unroll
      for (int i = 0; i < 16; i += 2) {
        const f32x2 ur = {Ur[m][i], Ur[m][i + 1]}, ui = {Ui[m][i], Ui[m][i + 1]};
        const f32x2 nr = __builtin_elementwise_fma(lr2, er, __builtin_elementwise_fma(-li2, ei, ur));
        const f32x2 ni = __builtin_elementwise_fma(lr2, ei, __builtin_elementwise_fma(li2, er, ui));
        er = nr; ei = ni;
        hr[i >> 1] = __builtin_elementwise_fma(cr2, er, -(ci2 * ei));
        hi[i >> 1] = -__builtin_elementwise_fma(cr2, ei, ci2 * er);
      }
#pragma unroll
      for (int c = 0; c < 2; ++c) {
        wre[c][m] = (u32x4){pk2(hr[0][c], hr[1][c]), pk2(hr[2][c], hr[3][c]), pk2(hr[4][c], hr[5][c]), pk2(hr[6][c], hr[7][c])};
        wim[c][m] = (u32x4){pk2(hi[0][c], hi[1][c]), pk2(hi[2][c], hi[3][c]), pk2(hi[4][c], hi[5][c]), pk2(hi[6][c], hi[7][c])};
      }
    }
    STAMP(5);
    {
      const rsrc_t hrs = mk_rsrc(ws + WS_H, 2048 * 4096);
      LAS char* sc = sm + SC_OFF + wave * 5120;
      LAS char* wp = sc + c32 * 144 + 64 * h;
      LAS char* rp = sc + (lane >> 3) * 144 + (lane & 7) * 16;
      const int hbase = (cg * 64 + 32 * wn + (lane >> 3)) * 4096 + rt * 512 + wm * 128 + (lane & 7) * 16;
#pragma unroll
      for (int ri = 0; ri < 2; ++ri) {
#pragma unroll
        for (int c = 0; c < 2; ++c)
#pragma unroll
          for (int m = 0; m < 2; ++m) *(LAS u32x4*)(wp + 32 * c + 16 * m) = ri ? wim[c][m] : wre[c][m];
        wait_lgkm0();
        u32x4 v[4];
#pragma unroll
        for (int j = 0; j < 4; ++j) v[j] = *(LAS u32x4*)(rp + j * 8 * 144);
#pragma unroll
        for (int j = 0; j < 4; ++j) __builtin_amdgcn_raw_buffer_store_b128(v[j], hrs, hbase + (ri * 1024 + 8 * j) * 4096, 0, 16);
        wait_lgkm0();
      }
    }
    STAMP(15);
    wait_vm<0>();
    STAMP(16);
    if (lane == 0) __hip_atomic_store(flagp(ws, FL_H + ((2 * rt + (wave >> 2)) * 16 + cg) * 64 + (wave & 3) * 8), tag, RLX_AGENT);
    phase2<false>(sm, ws, Bx, Dskip, out, rt2, ct2, wave, lane, g_st);
  }
}

extern "C" void kernel_launch(void* const* d_in, const int* in_sizes, int n_in, void* d_out, int out_size, void* d_ws,
                              size_t ws_size, hipStream_t stream) {
  fused_kernel<<<256, 768, 0, stream>>>((const u16*)d_in[0], (const u16*)d_in[1], (const u16*)d_in[2], (const u16*)d_in[3],
                                        (const u16*)d_in[4], (const u16*)d_in[5], (const u16*)d_in[6], (const u16*)d_in[7],
                                        (const u16*)d_in[8], (const u16*)d_in[9], (const u16*)d_in[10], (const u16*)d_in[11],
                                        (const u16*)d_in[12], (float*)d_out, (char*)d_ws);
}
```

```cpp
#include <hip/hip_runtime.h>
#include <stdint.h>

#define LAS __attribute__((address_space(3)))
#define GAS __attribute__((address_space(1)))
#define DI __device__ __forceinline__

typedef unsigned short u16;
typedef unsigned long long u64;
typedef __bf16 bf16x8 __attribute__((ext_vector_type(8)));
typedef __bf16 bf16x2 __attribute__((ext_vector_type(2)));
typedef short s16x4 __attribute__((ext_vector_type(4)));
typedef short s16x8 __attribute__((ext_vector_type(8)));
typedef float f32x2 __attribute__((ext_vector_type(2)));
typedef float f32x4 __attribute__((ext_vector_type(4)));
typedef float f32x16 __attribute__((ext_vector_type(16)));
typedef unsigned u32x2 __attribute__((ext_vector_type(2)));
typedef unsigned u32x4 __attribute__((ext_vector_type(4)));

#define MFMA32(a, b, c) __builtin_amdgcn_mfma_f32_32x32x16_bf16((a), (b), (c), 0, 0, 0)
#define RLX_AGENT __ATOMIC_RELAXED, __HIP_MEMORY_SCOPE_AGENT

constexpr size_t WS_O    = 0;
constexpr size_t WS_H    = 4u << 20;
constexpr size_t WS_KVP  = 12u << 20;
constexpr int    KVP_BYTES = 8448;
constexpr size_t WS_AGG  = 15u << 20;
constexpr size_t WS_FLAG = 16u << 20;
constexpr int FL_EPOCH_A = 0, FL_EPOCH_B = 256;
constexpr int FL_KV = 4096;
constexpr int FL_O = 32768;
constexpr int FL_H = 65536;
constexpr int LDS_BYTES = 160 * 1024;
constexpr unsigned SPIN_CAP = 1u << 20;
#define WARM_ON 2
#define WARMA 0
#define POLL_SLEEP 2
#define ROT2 1
#define OUT_AUX 2
#define ROTA 0
#define ROTB 9
#define P2S 4

#define STAMP_FLUSH2() do {} while (0)
#define STAMP(i) do {} while (0)
#define STAMP_FLUSH() do {} while (0)

DI float bf2f(u16 v) { return __uint_as_float((unsigned)v << 16); }
DI float bflo(unsigned v) { return __uint_as_float(v << 16); }
DI float bfhi(unsigned v) { return __uint_as_float(v & 0xffff0000u); }
DI unsigned pk2(float lo, float hi) { f32x2 x = {lo, hi}; return __builtin_bit_cast(unsigned, __builtin_convertvector(x, bf16x2)); }
template <int S> DI bf16x8 pack8(const f32x16& x) {
  u32x4 p = {pk2(x[8 * S], x[8 * S + 1]), pk2(x[8 * S + 2], x[8 * S + 3]), pk2(x[8 * S + 4], x[8 * S + 5]), pk2(x[8 * S + 6], x[8 * S + 7])};
  return __builtin_bit_cast(bf16x8, p);
}
template <int N> DI void wait_vm() { asm volatile("s_waitcnt vmcnt(%0)" ::"i"(N) : "memory"); }
DI void wait_lgkm0() { asm volatile("s_waitcnt lgkmcnt(0)" ::: "memory"); }
DI void bar() { __builtin_amdgcn_s_barrier(); }
DI void cfence() { asm volatile("" ::: "memory"); }
DI void sync_lds() { wait_lgkm0(); bar(); cfence(); }
DI bf16x8 lds_b128(LAS char* p) { return *(LAS bf16x8*)p; }
DI s16x4 lds_tr(LAS char* p) { return __builtin_amdgcn_ds_read_tr16_b64_v4i16((LAS s16x4*)p); }
DI bf16x8 cat8(s16x4 lo, s16x4 hi) { return __builtin_bit_cast(bf16x8, __builtin_shufflevector(lo, hi, 0, 1, 2, 3, 4, 5, 6, 7)); }
DI bf16x8 zero8() { s16x8 z = {0, 0, 0, 0, 0, 0, 0, 0}; return __builtin_bit_cast(bf16x8, z); }
DI GAS u64* flagp(char* ws, int off) { return (GAS u64*)(ws + WS_FLAG + off); }
DI float hmax(float v) { auto r = __builtin_amdgcn_permlane32_swap(__float_as_uint(v), __float_as_uint(v), false, false); return fmaxf(__uint_as_float(r[0]), __uint_as_float(r[1])); }
DI float hmin(float v) { auto r = __builtin_amdgcn_permlane32_swap(__float_as_uint(v), __float_as_uint(v), false, false); return fminf(__uint_as_float(r[0]), __uint_as_float(r[1])); }
DI float hsum(float v) { auto r = __builtin_amdgcn_permlane32_swap(__float_as_uint(v), __float_as_uint(v), false, false); return __uint_as_float(r[0]) + __uint_as_float(r[1]); }

#define SCHED() __builtin_amdgcn_sched_barrier(0)
template <int I> struct IC { static constexpr int v = I; };
template <int B, int E, class F> DI void sfor(F f) { if constexpr (B < E) { f(IC<B>{}); sfor<B + 1, E>(f); } }
typedef __amdgpu_buffer_rsrc_t rsrc_t;
DI rsrc_t mk_rsrc(const void* p, int bytes) { return __builtin_amdgcn_make_buffer_rsrc((void*)p, 0, bytes, 0x00020000); }
DI void dma16(rsrc_t r, LAS char* l, unsigned voff, int soff) {
  asm volatile("s_mov_b32 m0, %0\n\ts_nop 0\n\tbuffer_load_dwordx4 %1, %2, %3 offen lds"
               ::"s"((unsigned)(size_t)l), "v"(voff), "s"(r), "s"(soff) : "memory");
}
DI void l2touch(rsrc_t r, unsigned voff, int soff) {
  asm volatile("buffer_load_dword v167, %0, %1, %2 offen" ::"v"(voff), "s"(r), "s"(soff) : "memory", "v167");
}
template <int NM, int NR, int RPM, class M, class R>
DI void mgroup(M mma, R rd) {
  sfor<0, NM>([&](auto i) {
    constexpr int I = decltype(i)::v;
    mma(i); SCHED();
    sfor<0, RPM>([&](auto j) { constexpr int X = I * RPM + decltype(j)::v; if constexpr (X < NR) rd(IC<X>{}); });
    SCHED();
  });
}

template <bool NORM> DI void dual_exp(const f32x16& x, f32x16& pp, f32x16& pm) {
  float mx = fmaxf(x[0], x[1]), mn = fminf(x[0], x[1]);
#pragma unroll
  for (int i = 2; i < 16; i += 2) { mx = fmaxf(fmaxf(mx, x[i]), x[i + 1]); mn = fminf(fminf(mn, x[i]), x[i + 1]); }
  mx = hmax(mx); mn = hmin(mn);
  const float L2E = 1.4426950408889634f;
  const f32x2 l2 = {L2E, L2E}, nmx = {-mx * L2E, -mx * L2E}, pmn = {mn * L2E, mn * L2E};
  f32x2 sp = {0.f, 0.f}, sm = {0.f, 0.f};
#pragma unroll
  for (int i = 0; i < 16; i += 2) {
    const f32x2 xi = {x[i], x[i + 1]};
    const f32x2 a = __builtin_elementwise_fma(xi, l2, nmx), b = __builtin_elementwise_fma(xi, -l2, pmn);
    const f32x2 ea = {__builtin_amdgcn_exp2f(a[0]), __builtin_amdgcn_exp2f(a[1])};
    const f32x2 eb = {__builtin_amdgcn_exp2f(b[0]), __builtin_amdgcn_exp2f(b[1])};
    pp[i] = ea[0]; pp[i + 1] = ea[1]; pm[i] = eb[0]; pm[i + 1] = eb[1];
    if constexpr (NORM) { sp += ea; sm += eb; }
  }
  if constexpr (NORM) {
    const float rp = __builtin_amdgcn_rcpf(hsum(sp[0] + sp[1])), rm = __builtin_amdgcn_rcpf(hsum(sm[0] + sm[1]));
    const f32x2 rp2 = {rp, rp}, rm2 = {rm, rm};
#pragma unroll
    for (int i = 0; i < 16; i += 2) {
      f32x2 a = {pp[i], pp[i + 1]}, b = {pm[i], pm[i + 1]};
      a *= rp2; b *= rm2;
      pp[i] = a[0]; pp[i + 1] = a[1]; pm[i] = b[0]; pm[i + 1] = b[1];
    }
  }
}

DI void poll_flag(GAS u64* p, u64 tag) {
#pragma unroll 1
  for (unsigned spins = 0; spins < SPIN_CAP; ++spins) {
    const u64 v = __hip_atomic_load(p, RLX_AGENT);
    if (__all(v == tag)) break;
    __builtin_amdgcn_s_sleep(POLL_SLEEP);
  }
}

template <bool IS_A>
DI void phase2_load(LAS char* sm, char* ws, const u16* __restrict__ W0, const u16* __restrict__ W1, int rt2, int ct2, int lw, int lane,
                    int rot, u64 tag, bool upd_epoch, unsigned (&g_st)[2]) {
  constexpr int NT = IS_A ? 16 : 32;
  constexpr int S = P2S, SB = 32768;
  const int r5 = 8 * lw + (lane >> 3);
  const unsigned voffR = (unsigned)r5 * 2048 + (unsigned)(((lane & 7) ^ ((r5 >> 1) & 7)) * 16);
  const unsigned voffT = (unsigned)r5 * (IS_A ? 2048 : 4096) + (unsigned)(((lane & 7) ^ (((r5 >> 1) & 1) << 2)) * 16);
  const char* gAct = IS_A ? ws + WS_O + (size_t)rt2 * 128 * 64 : ws + WS_H + (size_t)rt2 * 256;
  const int actBytes = IS_A ? 2048 * 2048 - rt2 * 128 * 64 : 2048 * 4096 - rt2 * 256;
  const int oc = (lane & 7) ^ ((r5 >> 1) & 7);
  const unsigned voffO = (unsigned)(oc >> 2) * (2048 * 64) + (unsigned)r5 * 64 + (unsigned)(oc & 3) * 16;
  const char* gW0 = IS_A ? (const char*)W0 + (size_t)ct2 * 256 : (const char*)W0 + (size_t)ct2 * 128 * 2048;
  const char* gW1 = IS_A ? gW0 : (const char*)W1 + (size_t)ct2 * 128 * 2048;
  const int wBytes = IS_A ? 1024 * 2048 - ct2 * 256 : 128 * 2048;
  LAS char* dst0 = sm + lw * 1024;
  auto issAct = [&](int kt, int buf) {
    const rsrc_t r = mk_rsrc(gAct, kt < NT ? actBytes : 0);
    kt = (kt + rot) & (NT - 1);
#pragma unroll
    for (int i = 0; i < 4; ++i) {
      if constexpr (IS_A) dma16(r, dst0 + buf + i * 4096, voffO, (kt * 2 * 2048 + i * 32) * 64);
      else dma16(r, dst0 + buf + i * 4096, voffT, (kt * 64 + (i & 1) * 32) * 4096 + (i >> 1) * 128);
    }
  };
  auto issW = [&](int kt, int buf) {
    const bool live = kt < NT;
    kt = (kt + rot) & (NT - 1);
    const rsrc_t r = mk_rsrc((IS_A || kt < 16) ? gW0 : gW1, live ? wBytes : 0);
#pragma unroll
    for (int i = 0; i < 4; ++i) {
      if constexpr (IS_A) dma16(r, dst0 + buf + 16384 + i * 4096, voffT, (kt * 64 + (i & 1) * 32) * 2048 + (i >> 1) * 128);
      else dma16(r, dst0 + buf + 16384 + i * 4096, voffR, (kt & 15) * 128 + i * 32 * 2048);
    }
  };
  STAMP(10);
#pragma unroll
  for (int s = 0; s < S - 1; ++s) issW(s, s * SB);
  STAMP(11);
  if (lw == 0) {
    poll_flag(flagp(ws, (IS_A ? FL_O : FL_H) + rt2 * 128 + (lane & 15) * 8), tag);
    STAMP(12);
    __builtin_amdgcn_fence(__ATOMIC_ACQUIRE, "agent");
    wait_vm<0>();
    STAMP(13);
    if (upd_epoch && lane == 0) __hip_atomic_store(flagp(ws, IS_A ? FL_EPOCH_A : FL_EPOCH_B), tag, RLX_AGENT);
  }
  cfence(); bar(); cfence();
#pragma unroll
  for (int s = 0; s < S - 1; ++s) issAct(s, s * SB);
  STAMP(15);
  int wr = (S - 1) * SB;
#pragma unroll 1
  for (int t = 0; t < NT; ++t) {
    if (t < S - 2) { if (t == 0) wait_vm<4 * (S - 2)>(); else if (t == 1) wait_vm<4 * (S - 2) + 4>(); else wait_vm<4 * (S - 2) + 8>(); }
    else wait_vm<8 * (S - 2)>();
    bar(); cfence();
    issAct(t + S - 1, wr); issW(t + S - 1, wr);
    wr += SB; if (wr == S * SB) wr = 0;
  }
  wait_vm<0>();
  STAMP(16);
  STAMP_FLUSH2();
}

DI void publish_half(LAS unsigned* cnt, GAS u64* flag, u64 tag, int lane) {
  if (lane == 0) {
    const unsigned old = __hip_atomic_fetch_add(cnt, 1u, __ATOMIC_RELAXED, __HIP_MEMORY_SCOPE_WORKGROUP);
    if (old == 3u) __hip_atomic_store(flag, tag, RLX_AGENT);
  }
}

template <bool IS_A>
DI void phase2(LAS char* sm, char* ws, const u16* __restrict__ Bx, const u16* __restrict__ Dskip, float* __restrict__ out, int rt2, int ct2,
               int wave, int lane, unsigned (&g_st)[2]) {
  char* g_ws = ws;
  constexpr int NT = IS_A ? 16 : 32;
  constexpr int S = P2S, SB = 32768;
  const int h = lane >> 5, wr_ = wave >> 2, wt = wave & 3;
  const int row0 = rt2 * 128 + (IS_A ? 64 * wr_ : 32 * wt) + (lane >> 3);
  const int col0 = ct2 * 128 + (IS_A ? 32 * wt : 64 * wr_) + 4 * (lane & 7);
  u32x2 xr[2][4], dr[2];
  if constexpr (!IS_A) {
#pragma unroll
    for (int j = 0; j < 2; ++j) {
      dr[j] = *(const u32x2*)(Dskip + col0 + 32 * j);
#pragma unroll
      for (int i = 0; i < 4; ++i) xr[j][i] = *(const u32x2*)(Bx + (size_t)(row0 + 8 * i) * 1024 + col0 + 32 * j);
    }
  }
  cfence(); bar(); cfence();
  STAMP(19);
  const int xs = (lane >> 1) & 7;
  const int q = (lane & 15) >> 2, p4 = lane & 3, blk = (lane >> 4) & 1;
  int rofs[4];
#pragma unroll
  for (int s = 0; s < 4; ++s) rofs[s] = (IS_A ? 0 : 16384) + 64 * wr_ * 128 + (lane & 31) * 128 + 16 * ((2 * s + h) ^ xs);
  const int tofs = (IS_A ? 16384 + (16 * h + q) * 128 : (8 * h + q) * 128) + (wt >> 1) * 8192 + 32 * blk + 8 * p4 + ((64 * (wt & 1)) ^ (64 * ((q >> 1) & 1)));
  f32x16 acc[2];
#pragma unroll
  for (int j = 0; j < 2; ++j)
#pragma unroll
    for (int e = 0; e < 16; ++e) acc[j][e] = 0.f;
  {
    bf16x8 fr[2][2][2];
    s16x4 tl[2][2][2];
#pragma unroll
    for (int i = 0; i < 2; ++i) { fr[1][i][0] = zero8(); fr[1][i][1] = zero8(); tl[1][i][0] = (s16x4){0, 0, 0, 0}; tl[1][i][1] = (s16x4){0, 0, 0, 0}; }
    auto mma = [&](auto B_, auto I) {
      constexpr int B = decltype(B_)::v, i = decltype(I)::v, ks = i >> 1, j = i & 1;
      if constexpr (IS_A) acc[j] = MFMA32(cat8(tl[B][ks][0], tl[B][ks][1]), fr[B][ks][j], acc[j]);
      else acc[j] = MFMA32(fr[B][ks][j], cat8(tl[B][ks][0], tl[B][ks][1]), acc[j]);
    };
    auto rdf = [&](auto B_, auto G_, auto X, LAS char* sb) {
      constexpr int B = decltype(B_)::v, x = decltype(X)::v, ks = x >> 2, s = 2 * decltype(G_)::v + ks, y = x & 3;
      constexpr int ts = IS_A ? (s >> 1) * 4096 + (s & 1) * 512 : s * 2048, tt = IS_A ? 1024 : 512;
      if constexpr (y == 0) fr[B][ks][0] = lds_b128(sb + rofs[s]);
      else if constexpr (y < 3) tl[B][ks][y - 1] = lds_tr(sb + tofs + ts + (y - 1) * tt);
      else fr[B][ks][1] = lds_b128(sb + rofs[s] + 4096);
    };
    int rd = 0;
#pragma unroll 1
    for (int t = 0; t < NT; ++t) {
      wait_lgkm0(); bar(); cfence(); SCHED();
      LAS char* sb = sm + rd;
      mgroup<4, 8, 3>([&](auto i) { mma(IC<1>{}, i); }, [&](auto x) { rdf(IC<0>{}, IC<0>{}, x, sb); });
      mgroup<4, 8, 3>([&](auto i) { mma(IC<0>{}, i); }, [&](auto x) { rdf(IC<1>{}, IC<1>{}, x, sb); });
      rd += SB; if (rd == S * SB) rd = 0;
    }
    sfor<0, 4>([&](auto i) { mma(IC<1>{}, i); });
  }
  STAMP(20);
  {
    const rsrc_t ors = mk_rsrc(out, 2048 * 2048 * 4);
    const int obase = (row0 * 2048 + (IS_A ? 0 : 1024) + col0) * 4;
    LAS char* sc = sm + 131072 + wave * 4096;
    LAS char* wp = sc + (lane & 31) * 128;
    LAS char* rp = sc + (lane >> 3) * 128 + (((lane & 7) ^ (lane >> 3)) * 16);
#pragma unroll
    for (int j = 0; j < 2; ++j) {
#pragma unroll
      for (int g = 0; g < 4; ++g)
        *(LAS f32x4*)(wp + (((2 * g + h) ^ (lane & 7)) * 16)) = (f32x4){acc[j][4 * g], acc[j][4 * g + 1], acc[j][4 * g + 2], acc[j][4 * g + 3]};
      wait_lgkm0();
      f32x4 v[4];
#pragma unroll
      for (int i = 0; i < 4; ++i) v[i] = *(LAS f32x4*)(rp + i * 1024);
#pragma unroll
      for (int i = 0; i < 4; ++i) {
        if constexpr (!IS_A) {
          v[i][0] = fmaf(bflo(xr[j][i][0]), bflo(dr[j][0]), v[i][0]); v[i][1] = fmaf(bfhi(xr[j][i][0]), bfhi(dr[j][0]), v[i][1]);
          v[i][2] = fmaf(bflo(xr[j][i][1]), bflo(dr[j][1]), v[i][2]); v[i][3] = fmaf(bfhi(xr[j][i][1]), bfhi(dr[j][1]), v[i][3]);
        }
        __builtin_amdgcn_raw_buffer_store_b128(__builtin_bit_cast(u32x4, v[i]), ors, obase + (IS_A ? j * 32 * 8192 : j * 128) + i * 8 * 8192, 0, OUT_AUX);
      }
      wait_lgkm0();
    }
  }
  STAMP(21);
}

__global__ __launch_bounds__(768, 3) void fused_kernel(
    const u16* __restrict__ Ax, const u16* __restrict__ Wqkv, const u16* __restrict__ Wphi, const u16* __restrict__ Wo,
    const u16* __restrict__ Bx, const u16* __restrict__ Lre, const u16* __restrict__ Lim, const u16* __restrict__ Bre,
    const u16* __restrict__ Bim, const u16* __restrict__ Cre, const u16* __restrict__ Cim, const u16* __restrict__ Logdt,
    const u16* __restrict__ Dskip, float* __restrict__ out, char* ws) {
  __shared__ __attribute__((aligned(1024))) char smem[LDS_BYTES];
  LAS char* sm = (LAS char*)smem;
  char* g_ws = ws;
  unsigned g_st[2] = {0u, 0u};
  STAMP(0);
  const int tid = threadIdx.x, lane = tid & 63, wave = __builtin_amdgcn_readfirstlane(tid >> 6);
  const int b = blockIdx.x, xcd = b & 7, idx = b >> 3, i16 = idx >> 1;
  const bool isA = (idx & 1) == 0;
  const int rt = 4 * (xcd & 1) + (i16 & 3), cg = 4 * (xcd >> 1) + (i16 >> 2);
  const int rt2 = 4 * (xcd & 3) + (i16 & 3), ct2 = 4 * (xcd >> 2) + (i16 >> 2);
  const int h = lane >> 5, c32 = lane & 31;
  const int rotx = 4 * (xcd >> 1) + 8 * (xcd & 1);
  const int rot1 = (isA ? ROTA : ROTB) == 9 ? rotx : (isA ? ROTA : ROTB) * xcd;
  constexpr int WR_OFF = 98304;
  constexpr int PH_PITCH = 136, PH_BYTES = 64 * PH_PITCH;
  constexpr int RED_OFF = 8 * PH_BYTES;
  constexpr int RED_W = 8192 + 256;
  constexpr int RED_GAP = 6912;
  constexpr int KVF_OFF = 147456;
  constexpr int CNT_OFF = 163824;
  constexpr int SC_OFF = 98304;
  constexpr int SB1 = 49152;
  constexpr int TE_OFF = 3 * SB1;
  constexpr int TG_OFF = TE_OFF + 4096;
  constexpr int TC_OFF = TG_OFF + 4096;

  if (wave >= 8) {
    const int lw = wave - 8;
    const int r5 = 8 * lw + (lane >> 3);
    const unsigned rowchunk = (unsigned)(((lane & 7) ^ ((r5 >> 1) & 7)) * 16);
    LAS char* dst0 = sm + lw * 1024;
    if (isA) {
      const unsigned voffA = (unsigned)r5 * 2048 + rowchunk;
      const unsigned voffB = (unsigned)r5 * 6144 + (unsigned)(((lane & 7) ^ (((r5 >> 1) & 1) << 2)) * 16);
      const char* gA = (const char*)Ax + (size_t)rt * 256 * 2048;
      const char* gB = (const char*)Wqkv + (size_t)cg * 128;
      auto issX = [&](int kt, int buf) {
        const rsrc_t r = mk_rsrc(gA, kt < 16 ? 256 * 2048 : 0);
#pragma unroll
        for (int i = 0; i < 8; ++i) dma16(r, dst0 + buf + i * 4096, voffA, ((kt + rot1) & 15) * 128 + i * 32 * 2048);
      };
      auto issW = [&](int j, int kt, int u) {
        const rsrc_t r = mk_rsrc(gB, kt < 16 ? 1024 * 6144 - cg * 128 : 0);
#pragma unroll
        for (int i = 0; i < 2; ++i) dma16(r, dst0 + WR_OFF + (u & 7) * 8192 + i * 4096, voffB, (((kt + rot1) & 15) * 64 + i * 32) * 6144 + j * 2048);
      };
      issX(0, 0); issW(0, 0, 0); issW(1, 0, 1); issW(2, 0, 2);
      u64 epoch = __hip_atomic_load(flagp(ws, FL_EPOCH_A), RLX_AGENT);
      u16 wraw[8];
#pragma unroll
      for (int j = 0; j < 8; ++j) wraw[j] = Wphi[(size_t)(2 * cg + (lw >> 1)) * 1024 + (16 * (lw & 1) + 8 * (j >> 2) + 4 * h + (j & 3)) * 32 + c32];
      cfence();
      issX(1, 32768); issW(0, 1, 3); issW(1, 1, 4);
      int xw = 65536, u0 = 0;
#pragma unroll 1
      for (int t = 0; t < 16; ++t) {
        wait_vm<12>(); bar(); cfence();
        issW(2, t + 1, u0 + 5); issX(t + 2, xw); issW(0, t + 2, u0 + 6); issW(1, t + 2, u0 + 7);
        xw += 32768; if (xw == 98304) xw = 0;
        u0 = (u0 + 3) & 7;
      }
      wait_vm<0>();
      {
        s16x8 w8;
#pragma unroll
        for (int j = 0; j < 8; ++j) w8[j] = (short)wraw[j];
        *(LAS s16x8*)(sm + WR_OFF + 2 * 8192 + lw * 1024 + lane * 16) = w8;
      }
      const u64 tag = epoch + 1ull;
      sync_lds();
      __builtin_amdgcn_s_setprio(3);
      bar(); cfence();
      if (tid == 512) *(LAS u32x4*)(sm + CNT_OFF) = (u32x4){0u, 0u, 0u, 0u};
      STAMP(3);
      {
        const rsrc_t kvrs = mk_rsrc(ws + WS_KVP + (size_t)(cg * 8 + rt) * KVP_BYTES, KVP_BYTES);
#pragma unroll
        for (int j = 0; j < 3; ++j) {
          if (j < 2 || lw == 0) {
            const int zl = lane & 15, zh = zl >> 3, zks = (zl >> 1) & 3, zhh = zl & 1;
            f32x4 s0 = {0.f, 0.f, 0.f, 0.f}, s1 = {0.f, 0.f, 0.f, 0.f};
#pragma unroll
            for (int w = 0; w < 4; ++w) {
              if (j < 2) {
                LAS char* rp = sm + RED_OFF + (2 * w + j) * RED_W + (2 * w + j >= 5 ? RED_GAP : 0) + (2 * lw) * 1024 + lane * 16;
                s0 += *(LAS f32x4*)rp; s1 += *(LAS f32x4*)(rp + 1024);
              } else {
                LAS char* rp = sm + RED_OFF + (2 * w + zh) * RED_W + (2 * w + zh >= 5 ? RED_GAP : 0) + 8192 + (((zks >> 1) * 2 + zhh) * 4 + 2 * (zks & 1)) * 16;
                s0 += *(LAS f32x4*)rp; s1 += *(LAS f32x4*)(rp + 16);
              }
            }
            const u32x4 d = {pk2(s0[0], s0[1]), pk2(s0[2], s0[3]), pk2(s1[0], s1[1]), pk2(s1[2], s1[3])};
            if (j < 2) __builtin_amdgcn_raw_buffer_store_b128(d, kvrs, ((j * 4 + lw) * 64 + lane) * 16, 0, 16);
            else if (lane < 16) __builtin_amdgcn_raw_buffer_store_b128(d, kvrs, 8192 + zl * 16, 0, 16);
          }
        }
        STAMP(4);
        wait_vm<0>();
        STAMP(5);
        if (lane == 0) __hip_atomic_store(flagp(ws, FL_KV + cg * 256 + rt * 32 + lw * 8), tag, RLX_AGENT);
        if (lw == 0) poll_flag(flagp(ws, FL_KV + cg * 256 + (lane & 31) * 8), tag);
      }
      STAMP(6);
      __builtin_amdgcn_s_setprio(0);
      sync_lds();
      bar(); cfence();
      phase2_load<true>(sm, ws, Wo, Wo, rt2, ct2, lw, lane, ROT2 * (4 * (xcd & 3) + 8 * (xcd >> 2)), tag, b == 0, g_st);
    } else {
      const int tloc = 32 * ((r5 >> 2) & 1) + 16 * (r5 & 1) + ((r5 & 3) >> 1) + 2 * (r5 >> 3);
      const unsigned voffA = (unsigned)tloc * 2048 + rowchunk;
      const unsigned voffB = (unsigned)r5 * 2048 + rowchunk;
      const char* gA = (const char*)Bx + (size_t)rt * 256 * 2048;
      const char* gBr = (const char*)Bre + (size_t)cg * 64 * 2048;
      const char* gBi = (const char*)Bim + (size_t)cg * 64 * 2048;
      auto issue = [&](int kt, int buf) {
        const bool live = kt < 16;
        kt = (kt + rot1) & 15;
        const rsrc_t ra = mk_rsrc(gA, live ? 256 * 2048 : 0), rr = mk_rsrc(gBr, live ? 64 * 2048 : 0), ri = mk_rsrc(gBi, live ? 64 * 2048 : 0);
#pragma unroll
        for (int i = 0; i < 8; ++i) dma16(ra, dst0 + buf + i * 4096, voffA, kt * 128 + ((i >> 1) * 64 + (i & 1) * 8) * 2048);
#pragma unroll
        for (int i = 8; i < 12; ++i) dma16(i < 10 ? rr : ri, dst0 + buf + i * 4096, voffB, kt * 128 + (i & 1) * 32 * 2048);
      };
      issue(0, 0);
      u64 epoch = __hip_atomic_load(flagp(ws, FL_EPOCH_B), RLX_AGENT);
      cfence();
      issue(1, SB1);
      int wr = 2 * SB1;
#pragma unroll 1
      for (int t = 0; t < 16; ++t) {
        wait_vm<12>(); bar(); cfence();
        issue(t + 2, wr);
        wr += SB1; if (wr == 3 * SB1) wr = 0;
      }
      wait_vm<0>();
      const u64 tag = epoch + 1ull;
      const unsigned tlo = (unsigned)tag, thi = (unsigned)(tag >> 32);
      STAMP(2);
      bar();
      if (tid == 512) *(LAS u32x4*)(sm + CNT_OFF) = (u32x4){0u, 0u, 0u, 0u};
      STAMP(3);
      {
        const rsrc_t agrs = mk_rsrc(ws + WS_AGG + (size_t)cg * 8192, rt * 1024);
        u32x4 gr[2];
#pragma unroll 1
        for (unsigned spins = 0; spins < SPIN_CAP; ++spins) {
          unsigned bad = 0;
#pragma unroll
          for (int k = 0; k < 2; ++k) gr[k] = __builtin_amdgcn_raw_buffer_load_b128(agrs, ((lw + 4 * k) * 64 + lane) * 16, 0, 16);
#pragma unroll
          for (int k = 0; k < 2; ++k) if (lw + 4 * k < rt) bad |= (gr[k][1] ^ tlo) | (gr[k][3] ^ thi);
          if (__all(bad == 0)) break;
          __builtin_amdgcn_s_sleep(POLL_SLEEP);
        }
#pragma unroll
        for (int k = 0; k < 2; ++k)
          *(LAS f32x2*)(sm + TG_OFF + ((lw + 4 * k) * 64 + lane) * 8) = (f32x2){__uint_as_float(gr[k][0]), __uint_as_float(gr[k][2])};
      }
      STAMP(4);
      sync_lds();
      phase2_load<false>(sm, ws, Cre, Cim, rt2, ct2, lw, lane, ROT2 * (8 * (xcd & 3) + 16 * (xcd >> 2)), tag, rt2 == 15 && ct2 == 0, g_st);
    }
    return;
  }

  const int wm = wave >> 1, wn = wave & 1;
  const int xs = (lane >> 1) & 7;
  const int q = (lane & 15) >> 2, p4 = lane & 3, blk = (lane >> 4) & 1;
  int rofs[4];
#pragma unroll
  for (int s = 0; s < 4; ++s) rofs[s] = c32 * 128 + 16 * ((2 * s + h) ^ xs);

  if (isA) {
    u64 epoch = __hip_atomic_load(flagp(ws, FL_EPOCH_A), RLX_AGENT);
    if (WARMA) {
      const int sl = (xcd * 16 + i16) * 8 + wave;
      const uintptr_t p0 = (uintptr_t)Wqkv, p1 = (uintptr_t)Ax;
      l2touch(mk_rsrc((const void*)(sl < 768 ? p0 : p1), (sl < 768 || WARMA > 1) ? 0x7fffffff : 0), (unsigned)lane * 128, (sl < 768 ? sl : 2 * (sl - 768)) * 8192);
      if (WARMA > 2) l2touch(mk_rsrc((const void*)p1, sl >= 768 ? 0x7fffffff : 0), (unsigned)lane * 128, (2 * (sl - 768) + 1) * 8192);
      wait_vm<0>();
    }
    const int tofs = WR_OFF + (8 * h + q) * 128 + 64 * (wn ^ ((q >> 1) & 1)) + 32 * blk + 8 * p4;
    f32x16 Qt[2], Kt[2], V[2];
#pragma unroll
    for (int m = 0; m < 2; ++m)
#pragma unroll
      for (int e = 0; e < 16; ++e) { Qt[m][e] = 0.f; Kt[m][e] = 0.f; V[m][e] = 0.f; }
    STAMP(1);
    {
      bf16x8 fx[2][2];
      s16x4 fw[2][3][2];
#pragma unroll
      for (int i = 0; i < 2; ++i) fx[1][i] = zero8();
#pragma unroll
      for (int j = 0; j < 3; ++j) { fw[1][j][0] = (s16x4){0, 0, 0, 0}; fw[1][j][1] = (s16x4){0, 0, 0, 0}; }
      auto mma = [&](auto B_, auto I) {
        constexpr int B = decltype(B_)::v, i = decltype(I)::v, m = i / 3, j = i % 3;
        const bf16x8 w = cat8(fw[B][j][0], fw[B][j][1]);
        if constexpr (j == 0) Qt[m] = MFMA32(w, fx[B][m], Qt[m]);
        else if constexpr (j == 1) Kt[m] = MFMA32(w, fx[B][m], Kt[m]);
        else V[m] = MFMA32(fx[B][m], w, V[m]);
      };
      int xr = 0, u0 = 0;
#pragma unroll 1
      for (int t = 0; t < 16; ++t) {
        wait_lgkm0(); bar(); cfence(); SCHED();
        LAS char* xb = sm + xr + wm * 8192;
        LAS char* wb[3] = {sm + tofs + (u0 & 7) * 8192, sm + tofs + ((u0 + 1) & 7) * 8192, sm + tofs + ((u0 + 2) & 7) * 8192};
        auto rdf = [&](auto B_, auto S_, auto X) {
          constexpr int B = decltype(B_)::v, s = decltype(S_)::v, x = decltype(X)::v;
          if constexpr (x == 0) fx[B][0] = lds_b128(xb + rofs[s]);
          else if constexpr (x < 7) fw[B][(x - 1) >> 1][(x - 1) & 1] = lds_tr(wb[(x - 1) >> 1] + s * 2048 + ((x - 1) & 1) * 512);
          else fx[B][1] = lds_b128(xb + rofs[s] + 4096);
        };
        mgroup<6, 8, 2>([&](auto i) { mma(IC<1>{}, i); }, [&](auto x) { rdf(IC<0>{}, IC<0>{}, x); });
        mgroup<6, 8, 2>([&](auto i) { mma(IC<0>{}, i); }, [&](auto x) { rdf(IC<1>{}, IC<1>{}, x); });
        mgroup<6, 8, 2>([&](auto i) { mma(IC<1>{}, i); }, [&](auto x) { rdf(IC<0>{}, IC<2>{}, x); });
        mgroup<6, 8, 2>([&](auto i) { mma(IC<0>{}, i); }, [&](auto x) { rdf(IC<1>{}, IC<3>{}, x); });
        xr += 32768; if (xr == 98304) xr = 0;
        u0 = (u0 + 3) & 7;
      }
      sfor<0, 6>([&](auto i) { mma(IC<1>{}, i); });
    }
    asm volatile("" : "+v"(epoch));
    const u64 tag = epoch + 1ull;
    STAMP(2);
    sync_lds();
    STAMP(3);
    const bf16x8 wf0 = lds_b128(sm + WR_OFF + 2 * 8192 + (2 * wn) * 1024 + lane * 16);
    const bf16x8 wf1 = lds_b128(sm + WR_OFF + 2 * 8192 + (2 * wn + 1) * 1024 + lane * 16);
    f32x16 zero16;
#pragma unroll
    for (int e = 0; e < 16; ++e) zero16[e] = 0.f;
    LAS char* ph = sm + wave * PH_BYTES;
#pragma unroll
    for (int m = 0; m < 2; ++m) {
      f32x16 pk = MFMA32(wf0, pack8<0>(Kt[m]), zero16);
      pk = MFMA32(wf1, pack8<1>(Kt[m]), pk);
      f32x16 pp, pm;
      dual_exp<true>(pk, pp, pm);
      LAS char* rowp = ph + (32 * m + c32) * PH_PITCH + 8 * h;
#pragma unroll
      for (int g = 0; g < 4; ++g) {
        u32x2 a = {pk2(pp[4 * g], pp[4 * g + 1]), pk2(pp[4 * g + 2], pp[4 * g + 3])};
        u32x2 c = {pk2(pm[4 * g], pm[4 * g + 1]), pk2(pm[4 * g + 2], pm[4 * g + 3])};
        *(LAS u32x2*)(rowp + 16 * g) = a;
        *(LAS u32x2*)(rowp + 64 + 16 * g) = c;
      }
    }
    wait_lgkm0();
    STAMP(4);
    f32x16 KV[2], Z[2];
    KV[0] = zero16; KV[1] = zero16; Z[0] = zero16; Z[1] = zero16;
    {
      s16x8 o8;
#pragma unroll
      for (int j = 0; j < 8; ++j) o8[j] = (short)0x3f80;
      const bf16x8 ones = __builtin_bit_cast(bf16x8, o8);
      LAS char* tp = ph + (4 * h + q) * PH_PITCH + 32 * blk + 8 * p4;
#pragma unroll
      for (int m = 0; m < 2; ++m) {
#pragma unroll
        for (int s = 0; s < 2; ++s) {
          const bf16x8 vb = s ? pack8<1>(V[m]) : pack8<0>(V[m]);
          LAS char* t0 = tp + (32 * m + 16 * s) * PH_PITCH;
#pragma unroll
          for (int fs = 0; fs < 2; ++fs) {
            const bf16x8 af = cat8(lds_tr(t0 + 64 * fs), lds_tr(t0 + 8 * PH_PITCH + 64 * fs));
            KV[fs] = MFMA32(af, vb, KV[fs]);
            Z[fs] = MFMA32(af, ones, Z[fs]);
          }
        }
      }
    }
    {
      LAS char* rw = sm + RED_OFF + wave * RED_W + (wave >= 5 ? RED_GAP : 0);
#pragma unroll
      for (int fs = 0; fs < 2; ++fs)
#pragma unroll
        for (int g = 0; g < 4; ++g) {
          f32x4 v = {KV[fs][4 * g], KV[fs][4 * g + 1], KV[fs][4 * g + 2], KV[fs][4 * g + 3]};
          *(LAS f32x4*)(rw + (fs * 4 + g) * 1024 + lane * 16) = v;
        }
      if (c32 == 0) {
#pragma unroll
        for (int fs = 0; fs < 2; ++fs)
#pragma unroll
          for (int g = 0; g < 4; ++g) {
            f32x4 z = {Z[fs][4 * g], Z[fs][4 * g + 1], Z[fs][4 * g + 2], Z[fs][4 * g + 3]};
            *(LAS f32x4*)(rw + 8192 + ((fs * 2 + h) * 4 + g) * 16) = z;
          }
      }
    }
    STAMP(5);
    sync_lds();
    STAMP(6);
    STAMP(9);
    bf16x8 fq[2][4];
#pragma unroll
    for (int m = 0; m < 2; ++m) {
      f32x16 pq = MFMA32(wf0, pack8<0>(Qt[m]), zero16);
      pq = MFMA32(wf1, pack8<1>(Qt[m]), pq);
      f32x16 pp, pm;
      dual_exp<false>(pq, pp, pm);
      fq[m][0] = pack8<0>(pp); fq[m][1] = pack8<1>(pp); fq[m][2] = pack8<0>(pm); fq[m][3] = pack8<1>(pm);
    }
    asm volatile("" : "+v"(fq[0][0]), "+v"(fq[0][1]), "+v"(fq[0][2]), "+v"(fq[0][3]), "+v"(fq[1][0]), "+v"(fq[1][1]), "+v"(fq[1][2]), "+v"(fq[1][3]));
    STAMP(10);
    cfence(); bar(); cfence();
    STAMP(12);
    {
      const rsrc_t kvrs = mk_rsrc(ws + WS_KVP + (size_t)cg * 8 * KVP_BYTES, 8 * KVP_BYTES);
      u32x4 v[8], vz[8];
#pragma unroll
      for (int r = 0; r < 8; ++r) v[r] = __builtin_amdgcn_raw_buffer_load_b128(kvrs, tid * 16, r * KVP_BYTES, 16);
      if (wave == 7) {
#pragma unroll
        for (int r = 0; r < 8; ++r) vz[r] = __builtin_amdgcn_raw_buffer_load_b128(kvrs, 8192 + (lane & 15) * 16, r * KVP_BYTES, 16);
      }
      auto sum8 = [&](const u32x4 (&x)[8]) {
        f32x4 a = {0.f, 0.f, 0.f, 0.f}, c = {0.f, 0.f, 0.f, 0.f};
#pragma unroll
        for (int r = 0; r < 8; ++r) {
          a += (f32x4){bflo(x[r][0]), bfhi(x[r][0]), bflo(x[r][1]), bfhi(x[r][1])};
          c += (f32x4){bflo(x[r][2]), bfhi(x[r][2]), bflo(x[r][3]), bfhi(x[r][3])};
        }
        return (u32x4){pk2(a[0], a[1]), pk2(a[2], a[3]), pk2(c[0], c[1]), pk2(c[2], c[3])};
      };
      *(LAS u32x4*)(sm + KVF_OFF + tid * 16) = sum8(v);
      if (wave == 7) { const u32x4 z = sum8(vz); if (lane < 16) *(LAS u32x4*)(sm + KVF_OFF + 8192 + lane * 16) = z; }
    }
    STAMP(13);
    sync_lds();
    STAMP(14);
    {
      bf16x8 kf[4], zf[4];
      s16x8 o8;
#pragma unroll
      for (int j = 0; j < 8; ++j) o8[j] = (short)0x3f80;
#pragma unroll
      for (int ks = 0; ks < 4; ++ks) {
        kf[ks] = lds_b128(sm + KVF_OFF + wn * 4096 + ks * 1024 + lane * 16);
        const bf16x8 z = lds_b128(sm + KVF_OFF + 8192 + wn * 128 + (ks * 2 + h) * 16);
        zf[ks] = (lane & 1) ? __builtin_bit_cast(bf16x8, o8) : z;
      }
      LAS char* sc = sm + SC_OFF + wave * 5120;
#pragma unroll
      for (int m = 0; m < 2; ++m) {
        f32x16 np = MFMA32(kf[0], fq[m][0], zero16), nm = MFMA32(kf[2], fq[m][2], zero16);
        f32x16 dp = MFMA32(zf[0], fq[m][0], zero16), dm = MFMA32(zf[2], fq[m][2], zero16);
        np = MFMA32(kf[1], fq[m][1], np); nm = MFMA32(kf[3], fq[m][3], nm);
        dp = MFMA32(zf[1], fq[m][1], dp); dm = MFMA32(zf[3], fq[m][3], dm);
        const float rp = __builtin_amdgcn_rcpf(dp[1]), rm = __builtin_amdgcn_rcpf(dm[1]);
        const float rinv = 1.0f / (dp[0] * rp + dm[0] * rm + 1e-6f);
        const float cp = rp * rinv, cm = rm * rinv;
        f32x16 o;
#pragma unroll
        for (int e = 0; e < 16; ++e) o[e] = np[e] * cp + nm[e] * cm;
        LAS char* op = sc + (32 * m + c32) * 80 + h * 32;
        *(LAS bf16x8*)op = pack8<0>(o);
        *(LAS bf16x8*)(op + 16) = pack8<1>(o);
      }
      wait_lgkm0();
      const rsrc_t ors = mk_rsrc(ws + WS_O, 2048 * 2048);
      const int obase = ((2 * cg + wn) * 2048 + rt * 256 + 64 * wm) * 64 + lane * 16;
#pragma unroll
      for (int j = 0; j < 4; ++j) {
        const u32x4 v = *(LAS u32x4*)(sc + (16 * j + (lane >> 2)) * 80 + (lane & 3) * 16);
        __builtin_amdgcn_raw_buffer_store_b128(v, ors, obase + j * 1024, 0, 16);
      }
    }
    STAMP(15);
    wait_vm<0>();
    STAMP(16);
    publish_half((LAS unsigned*)(sm + CNT_OFF) + (wave >> 2), flagp(ws, FL_O + (2 * rt + (wave >> 2)) * 128 + cg * 8), tag, lane);
    phase2<true>(sm, ws, Bx, Dskip, out, rt2, ct2, wave, lane, g_st);
  } else {
    u64 epoch = __hip_atomic_load(flagp(ws, FL_EPOCH_B), RLX_AGENT);
    const uintptr_t pw0 = (uintptr_t)Wqkv, pw1 = (uintptr_t)Cre, pw2 = (uintptr_t)Cim, pw3 = (uintptr_t)Wo;
    if (wave == 7) {
      const int ch = 64 * cg + lane;
      const float lamr = bf2f(Lre[ch]), lami = bf2f(Lim[ch]);
      const float dt = expf(bf2f(Logdt[ch]));
      const float zr = dt * lamr, zi = dt * lami;
      float sn, cs;
      sincosf(zi, &sn, &cs);
      const float mag = expf(zr);
      const float lr = mag * cs, li = mag * sn;
      const float nr = lr - 1.0f, ni = li, dd = 2.0f / (lamr * lamr + lami * lami);
      const float c2r = (nr * lamr + ni * lami) * dd, c2i = (ni * lamr - nr * lami) * dd;
      float pr = lr, pi = li;
#pragma unroll
      for (int i = 0; i < 4; ++i) { const float t = pr * pr - pi * pi; pi = 2.0f * pr * pi; pr = t; }
      const float l16r = pr, l16i = pi;
      { const float t = pr * pr - pi * pi; pi = 2.0f * pr * pi; pr = t; }
      const float l32r = pr, l32i = pi;
#pragma unroll
      for (int i = 0; i < 3; ++i) { const float t = pr * pr - pi * pi; pi = 2.0f * pr * pi; pr = t; }
      LAS f32x4* tc = (LAS f32x4*)(sm + TC_OFF + lane * 48);
      tc[0] = (f32x4){lr, li, c2r, c2i}; tc[1] = (f32x4){l16r, l16i, l32r, l32i}; tc[2] = (f32x4){pr, pi, 0.f, 0.f};
    }
    f32x16 Ur[2], Ui[2];
#pragma unroll
    for (int m = 0; m < 2; ++m)
#pragma unroll
      for (int e = 0; e < 16; ++e) { Ur[m][e] = 0.f; Ui[m][e] = 0.f; }
    STAMP(1);
    {
      bf16x8 fx[2][2], fb[2][2];
#pragma unroll
      for (int i = 0; i < 2; ++i) { fx[1][i] = zero8(); fb[1][i] = zero8(); }
      auto mma = [&](auto B_, auto I) {
        constexpr int B = decltype(B_)::v, i = decltype(I)::v;
        if constexpr (i == 0) Ur[0] = MFMA32(fx[B][0], fb[B][0], Ur[0]);
        else if constexpr (i == 1) Ui[0] = MFMA32(fx[B][0], fb[B][1], Ui[0]);
        else if constexpr (i == 2) Ur[1] = MFMA32(fx[B][1], fb[B][0], Ur[1]);
        else Ui[1] = MFMA32(fx[B][1], fb[B][1], Ui[1]);
      };
      int rd = 0;
#pragma unroll 1
      for (int t = 0; t < 16; ++t) {
        wait_lgkm0(); bar(); cfence(); SCHED();
        if (WARM_ON && (t & 7) == wave && t < 12 && (t >= 6 || WARM_ON > 1)) {
          const uintptr_t wb_ = t < 6 ? pw0 : (t < 8 ? pw1 : (t < 10 ? pw2 : pw3));
          l2touch(mk_rsrc((const void*)wb_, 0x7fffffff), (unsigned)lane * 128, ((t < 6 ? t : (t & 1)) * 128 + xcd * 16 + i16) * 8192);
        }
        LAS char* xb = sm + rd + wm * 8192;
        LAS char* bb = sm + rd + 32768 + wn * 4096;
        auto rdf = [&](auto B_, auto S_, auto X) {
          constexpr int B = decltype(B_)::v, s = decltype(S_)::v, x = decltype(X)::v;
          if constexpr (x == 0) fx[B][0] = lds_b128(xb + rofs[s]);
          else if constexpr (x == 1) fb[B][0] = lds_b128(bb + rofs[s]);
          else if constexpr (x == 2) fb[B][1] = lds_b128(bb + rofs[s] + 8192);
          else fx[B][1] = lds_b128(xb + rofs[s] + 4096);
        };
        mgroup<4, 4, 2>([&](auto i) { mma(IC<1>{}, i); }, [&](auto x) { rdf(IC<0>{}, IC<0>{}, x); });
        mgroup<4, 4, 2>([&](auto i) { mma(IC<0>{}, i); }, [&](auto x) { rdf(IC<1>{}, IC<1>{}, x); });
        mgroup<4, 4, 2>([&](auto i) { mma(IC<1>{}, i); }, [&](auto x) { rdf(IC<0>{}, IC<2>{}, x); });
        mgroup<4, 4, 2>([&](auto i) { mma(IC<0>{}, i); }, [&](auto x) { rdf(IC<1>{}, IC<3>{}, x); });
        rd += SB1; if (rd == 3 * SB1) rd = 0;
      }
      sfor<0, 4>([&](auto i) { mma(IC<1>{}, i); });
    }
    asm volatile("" : "+v"(epoch));
    const u64 tag = epoch + 1ull;
    const unsigned tlo = (unsigned)tag, thi = (unsigned)(tag >> 32);
    STAMP(2);
    const int seg = 2 * wm + h, chl = 32 * wn + c32;
    const f32x4 k0 = *(LAS f32x4*)(sm + TC_OFF + chl * 48), k1 = *(LAS f32x4*)(sm + TC_OFF + chl * 48 + 16), k2 = *(LAS f32x4*)(sm + TC_OFF + chl * 48 + 32);
    const f32x2 lr2 = {k0[0], k0[0]}, li2 = {k0[1], k0[1]}, cr2 = {k0[2], k0[2]}, ci2 = {k0[3], k0[3]};
    const float l16r = k1[0], l16i = k1[1], l32r = k1[2], l32i = k1[3], l256r = k2[0], l256i = k2[1];
    f32x2 er = {0.f, 0.f}, ei = {0.f, 0.f};
#pragma unroll
    for (int m = 0; m < 2; ++m)
#pragma unroll
      for (int i = 0; i < 16; i += 2) {
        const f32x2 ur = {Ur[m][i], Ur[m][i + 1]}, ui = {Ui[m][i], Ui[m][i + 1]};
        const f32x2 nr = __builtin_elementwise_fma(lr2, er, __builtin_elementwise_fma(-li2, ei, ur));
        const f32x2 ni = __builtin_elementwise_fma(lr2, ei, __builtin_elementwise_fma(li2, er, ui));
        er = nr; ei = ni;
      }
    const float e0r = er[0], e0i = ei[0];
    *(LAS f32x2*)(sm + TE_OFF + (seg * 64 + chl) * 8) =
        (f32x2){fmaf(l16r, e0r, fmaf(-l16i, e0i, er[1])), fmaf(l16r, e0i, fmaf(l16i, e0r, ei[1]))};
    STAMP(3);
    sync_lds();
    STAMP(7);
    f32x2 E[8];
#pragma unroll
    for (int s = 0; s < 8; ++s) E[s] = *(LAS f32x2*)(sm + TE_OFF + (s * 64 + chl) * 8);
    const rsrc_t agrs = mk_rsrc(ws + WS_AGG + (size_t)cg * 8192, 8192);
    if (wm == 0) {
      float tr = 0.f, ti = 0.f;
#pragma unroll
      for (int s = 0; s < 8; ++s) { const float nr = fmaf(l32r, tr, fmaf(-l32i, ti, E[s][0])), ni = fmaf(l32r, ti, fmaf(l32i, tr, E[s][1])); tr = nr; ti = ni; }
      if (h == 0) {
        const u32x4 gr = {__float_as_uint(tr), tlo, __float_as_uint(ti), thi};
        __builtin_amdgcn_raw_buffer_store_b128(gr, agrs, (rt * 64 + chl) * 16, 0, 16);
      }
    }
    STAMP(4);
    sync_lds();
    STAMP(6);
    float sr = 0.f, si = 0.f;
#pragma unroll 1
    for (int r = 0; r < rt; ++r) {
      const f32x2 g = *(LAS f32x2*)(sm + TG_OFF + (r * 64 + chl) * 8);
      const float nr = fmaf(l256r, sr, fmaf(-l256i, si, g[0])), ni = fmaf(l256r, si, fmaf(l256i, sr, g[1]));
      sr = nr; si = ni;
    }
#pragma unroll
    for (int s = 0; s < 7; ++s) {
      const float nr = fmaf(l32r, sr, fmaf(-l32i, si, E[s][0])), ni = fmaf(l32r, si, fmaf(l32i, sr, E[s][1]));
      if (s < seg) { sr = nr; si = ni; }
    }
    er = (f32x2){sr, fmaf(l16r, sr, fmaf(-l16i, si, e0r))};
    ei = (f32x2){si, fmaf(l16r, si, fmaf(l16i, sr, e0i))};
    u32x4 wre[2][2], wim[2][2];
#pragma unroll
    for (int m = 0; m < 2; ++m) {
      f32x2 hr[8], hi[8];
#pragma unroll
      for (int i = 0; i < 16; i += 2) {
        const f32x2 ur = {Ur[m][i], Ur[m][i + 1]}, ui = {Ui[m][i], Ui[m][i + 1]};
        const f32x2 nr = __builtin_elementwise_fma(lr2, er, __builtin_elementwise_fma(-li2, ei, ur));
        const f32x2 ni = __builtin_elementwise_fma(lr2, ei, __builtin_elementwise_fma(li2, er, ui));
        er = nr; ei = ni;
        hr[i >> 1] = __builtin_elementwise_fma(cr2, er, -(ci2 * ei));
        hi[i >> 1] = -__builtin_elementwise_fma(cr2, ei, ci2 * er);
      }
#pragma unroll
      for (int c = 0; c < 2; ++c) {
        wre[c][m] = (u32x4){pk2(hr[0][c], hr[1][c]), pk2(hr[2][c], hr[3][c]), pk2(hr[4][c], hr[5][c]), pk2(hr[6][c], hr[7][c])};
        wim[c][m] = (u32x4){pk2(hi[0][c], hi[1][c]), pk2(hi[2][c], hi[3][c]), pk2(hi[4][c], hi[5][c]), pk2(hi[6][c], hi[7][c])};
      }
    }
    STAMP(5);
    {
      const rsrc_t hrs = mk_rsrc(ws + WS_H, 2048 * 4096);
      LAS char* sc = sm + SC_OFF + wave * 5120;
      LAS char* wp = sc + c32 * 144 + 64 * h;
      LAS char* rp = sc + (lane >> 3) * 144 + (lane & 7) * 16;
      const int hbase = (cg * 64 + 32 * wn + (lane >> 3)) * 4096 + rt * 512 + wm * 128 + (lane & 7) * 16;
#pragma unroll
      for (int ri = 0; ri < 2; ++ri) {
#pragma unroll
        for (int c = 0; c < 2; ++c)
#pragma unroll
          for (int m = 0; m < 2; ++m) *(LAS u32x4*)(wp + 32 * c + 16 * m) = ri ? wim[c][m] : wre[c][m];
        wait_lgkm0();
        u32x4 v[4];
#pragma unroll
        for (int j = 0; j < 4; ++j) v[j] = *(LAS u32x4*)(rp + j * 8 * 144);
#pragma unroll
        for (int j = 0; j < 4; ++j) __builtin_amdgcn_raw_buffer_store_b128(v[j], hrs, hbase + (ri * 1024 + 8 * j) * 4096, 0, 16);
        wait_lgkm0();
      }
    }
    STAMP(15);
    wait_vm<0>();
    STAMP(16);
    publish_half((LAS unsigned*)(sm + CNT_OFF) + (wave >> 2), flagp(ws, FL_H + (2 * rt + (wave >> 2)) * 128 + cg * 8), tag, lane);
    phase2<false>(sm, ws, Bx, Dskip, out, rt2, ct2, wave, lane, g_st);
  }
}

extern "C" void kernel_launch(void* const* d_in, const int* in_sizes, int n_in, void* d_out, int out_size, void* d_ws,
                              size_t ws_size, hipStream_t stream) {
  fused_kernel<<<256, 768, 0, stream>>>((const u16*)d_in[0], (const u16*)d_in[1], (const u16*)d_in[2], (const u16*)d_in[3],
                                        (const u16*)d_in[4], (const u16*)d_in[5], (const u16*)d_in[6], (const u16*)d_in[7],
                                        (const u16*)d_in[8], (const u16*)d_in[9], (const u16*)d_in[10], (const u16*)d_in[11],
                                        (const u16*)d_in[12], (float*)d_out, (char*)d_ws);
}
```

```cpp
#include <hip/hip_runtime.h>
#include <stdint.h>

#define LAS __attribute__((address_space(3)))
#define GAS __attribute__((address_space(1)))
#define DI __device__ __forceinline__

typedef unsigned short u16;
typedef unsigned long long u64;
typedef __bf16 bf16x8 __attribute__((ext_vector_type(8)));
typedef __bf16 bf16x2 __attribute__((ext_vector_type(2)));
typedef short s16x4 __attribute__((ext_vector_type(4)));
typedef short s16x8 __attribute__((ext_vector_type(8)));
typedef float f32x2 __attribute__((ext_vector_type(2)));
typedef float f32x4 __attribute__((ext_vector_type(4)));
typedef float f32x16 __attribute__((ext_vector_type(16)));
typedef unsigned u32x2 __attribute__((ext_vector_type(2)));
typedef unsigned u32x4 __attribute__((ext_vector_type(4)));

#define MFMA32(a, b, c) __builtin_amdgcn_mfma_f32_32x32x16_bf16((a), (b), (c), 0, 0, 0)
#define MFMA16(a, b, c) __builtin_amdgcn_mfma_f32_16x16x32_bf16((a), (b), (c), 0, 0, 0)
#define RLX_AGENT __ATOMIC_RELAXED, __HIP_MEMORY_SCOPE_AGENT

constexpr size_t WS_O    = 0;
constexpr size_t WS_H    = 4u << 20;
constexpr size_t WS_KVP  = 12u << 20;
constexpr int    KVP_BYTES = 8448;
constexpr size_t WS_AGG  = 15u << 20;
constexpr size_t WS_FLAG = 16u << 20;
constexpr int FL_EPOCH_A = 0, FL_EPOCH_B = 256;
constexpr int FL_KV = 4096;
constexpr int FL_O = 32768;
constexpr int FL_H = 65536;
constexpr int LDS_BYTES = 160 * 1024;
constexpr unsigned SPIN_CAP = 1u << 20;
#define WARM_ON 2
#define POLL_SLEEP 2
#define P2RPM 1
#define ROT2 1
#define OUT_AUX 2
#define ROTA 0
#define ROTB 9
#define P2S 4

#define STAMP_FLUSH2() do {} while (0)
#define STAMP(i) do {} while (0)
#define STAMP_FLUSH() do {} while (0)

DI float bf2f(u16 v) { return __uint_as_float((unsigned)v << 16); }
DI float bflo(unsigned v) { return __uint_as_float(v << 16); }
DI float bfhi(unsigned v) { return __uint_as_float(v & 0xffff0000u); }
DI unsigned pk2(float lo, float hi) { f32x2 x = {lo, hi}; return __builtin_bit_cast(unsigned, __builtin_convertvector(x, bf16x2)); }
template <int S> DI bf16x8 pack8(const f32x16& x) {
  u32x4 p = {pk2(x[8 * S], x[8 * S + 1]), pk2(x[8 * S + 2], x[8 * S + 3]), pk2(x[8 * S + 4], x[8 * S + 5]), pk2(x[8 * S + 6], x[8 * S + 7])};
  return __builtin_bit_cast(bf16x8, p);
}
template <int N> DI void wait_vm() { asm volatile("s_waitcnt vmcnt(%0)" ::"i"(N) : "memory"); }
DI void wait_lgkm0() { asm volatile("s_waitcnt lgkmcnt(0)" ::: "memory"); }
DI void bar() { __builtin_amdgcn_s_barrier(); }
DI void cfence() { asm volatile("" ::: "memory"); }
DI void sync_lds() { wait_lgkm0(); bar(); cfence(); }
DI bf16x8 lds_b128(LAS char* p) { return *(LAS bf16x8*)p; }
DI s16x4 lds_tr(LAS char* p) { return __builtin_amdgcn_ds_read_tr16_b64_v4i16((LAS s16x4*)p); }
DI bf16x8 cat8(s16x4 lo, s16x4 hi) { return __builtin_bit_cast(bf16x8, __builtin_shufflevector(lo, hi, 0, 1, 2, 3, 4, 5, 6, 7)); }
DI bf16x8 zero8() { s16x8 z = {0, 0, 0, 0, 0, 0, 0, 0}; return __builtin_bit_cast(bf16x8, z); }
DI GAS u64* flagp(char* ws, int off) { return (GAS u64*)(ws + WS_FLAG + off); }
DI float vmax(float a, float b) { return __builtin_elementwise_maximum(a, b); }
DI float vmin(float a, float b) { return __builtin_elementwise_minimum(a, b); }
DI float hmax(float v) { auto r = __builtin_amdgcn_permlane32_swap(__float_as_uint(v), __float_as_uint(v), false, false); return vmax(__uint_as_float(r[0]), __uint_as_float(r[1])); }
DI float hmin(float v) { auto r = __builtin_amdgcn_permlane32_swap(__float_as_uint(v), __float_as_uint(v), false, false); return vmin(__uint_as_float(r[0]), __uint_as_float(r[1])); }
DI float hsum(float v) { auto r = __builtin_amdgcn_permlane32_swap(__float_as_uint(v), __float_as_uint(v), false, false); return __uint_as_float(r[0]) + __uint_as_float(r[1]); }

#define SCHED() __builtin_amdgcn_sched_barrier(0)
template <int I> struct IC { static constexpr int v = I; };
template <int B, int E, class F> DI void sfor(F f) { if constexpr (B < E) { f(IC<B>{}); sfor<B + 1, E>(f); } }
typedef __amdgpu_buffer_rsrc_t rsrc_t;
DI rsrc_t mk_rsrc(const void* p, int bytes) { return __builtin_amdgcn_make_buffer_rsrc((void*)p, 0, bytes, 0x00020000); }
DI void dma16(rsrc_t r, LAS char* l, unsigned voff, int soff) {
  asm volatile("s_mov_b32 m0, %0\n\ts_nop 0\n\tbuffer_load_dwordx4 %1, %2, %3 offen lds"
               ::"s"((unsigned)(size_t)l), "v"(voff), "s"(r), "s"(soff) : "memory");
}
DI void l2touch(rsrc_t r, unsigned voff, int soff) {
  asm volatile("buffer_load_dword v167, %0, %1, %2 offen" ::"v"(voff), "s"(r), "s"(soff) : "memory", "v167");
}
template <int NM, int NR, int RPM, class M, class R>
DI void mgroup(M mma, R rd) {
  sfor<0, NM>([&](auto i) {
    constexpr int I = decltype(i)::v;
    mma(i); SCHED();
    sfor<0, RPM>([&](auto j) { constexpr int X = I * RPM + decltype(j)::v; if constexpr (X < NR) rd(IC<X>{}); });
    SCHED();
  });
}

template <bool NORM> DI void dual_exp(const f32x16& x, f32x16& pp, f32x16& pm) {
  float mx = vmax(x[0], x[1]), mn = vmin(x[0], x[1]);
#pragma unroll
  for (int i = 2; i < 16; i += 2) { mx = vmax(vmax(mx, x[i]), x[i + 1]); mn = vmin(vmin(mn, x[i]), x[i + 1]); }
  mx = hmax(mx); mn = hmin(mn);
  const float L2E = 1.4426950408889634f;
  const f32x2 l2 = {L2E, L2E}, nmx = {-mx * L2E, -mx * L2E}, pmn = {mn * L2E, mn * L2E};
  f32x2 sp = {0.f, 0.f}, sm = {0.f, 0.f};
#pragma unroll
  for (int i = 0; i < 16; i += 2) {
    const f32x2 xi = {x[i], x[i + 1]};
    const f32x2 a = __builtin_elementwise_fma(xi, l2, nmx), b = __builtin_elementwise_fma(xi, -l2, pmn);
    const f32x2 ea = {__builtin_amdgcn_exp2f(a[0]), __builtin_amdgcn_exp2f(a[1])};
    const f32x2 eb = {__builtin_amdgcn_exp2f(b[0]), __builtin_amdgcn_exp2f(b[1])};
    pp[i] = ea[0]; pp[i + 1] = ea[1]; pm[i] = eb[0]; pm[i + 1] = eb[1];
    if constexpr (NORM) { sp += ea; sm += eb; }
  }
  if constexpr (NORM) {
    const float rp = __builtin_amdgcn_rcpf(hsum(sp[0] + sp[1])), rm = __builtin_amdgcn_rcpf(hsum(sm[0] + sm[1]));
    const f32x2 rp2 = {rp, rp}, rm2 = {rm, rm};
#pragma unroll
    for (int i = 0; i < 16; i += 2) {
      f32x2 a = {pp[i], pp[i + 1]}, b = {pm[i], pm[i + 1]};
      a *= rp2; b *= rm2;
      pp[i] = a[0]; pp[i + 1] = a[1]; pm[i] = b[0]; pm[i + 1] = b[1];
    }
  }
}

DI void poll_flag(GAS u64* p, u64 tag) {
#pragma unroll 1
  for (unsigned spins = 0; spins < SPIN_CAP; ++spins) {
    const u64 v = __hip_atomic_load(p, RLX_AGENT);
    if (__all(v == tag)) break;
    __builtin_amdgcn_s_sleep(POLL_SLEEP);
  }
}

template <bool IS_A>
DI void phase2_load(LAS char* sm, char* ws, const u16* __restrict__ W0, const u16* __restrict__ W1, int rt2, int ct2, int lw, int lane,
                    int rot, u64 tag, bool upd_epoch, unsigned (&g_st)[2]) {
  constexpr int NT = IS_A ? 16 : 32;
  constexpr int S = P2S, SB = 32768;
  const int r5 = 8 * lw + (lane >> 3);
  const unsigned voffR = (unsigned)r5 * 2048 + (unsigned)(((lane & 7) ^ ((r5 >> 1) & 7)) * 16);
  auto voffTw = [&](int w) {
    const int k = 8 * w + (lane >> 3), f2 = ((k >> 1) & 1) | (((k >> (IS_A ? 4 : 3)) & 1) << 1);
    return (unsigned)k * (IS_A ? 2048 : 4096) + (unsigned)(((lane & 7) ^ (f2 << 1)) * 16);
  };
  const unsigned voffT = voffTw(lw);
  const char* gAct = IS_A ? ws + WS_O + (size_t)rt2 * 128 * 64 : ws + WS_H + (size_t)rt2 * 256;
  const int actBytes = IS_A ? 2048 * 2048 - rt2 * 128 * 64 : 2048 * 4096 - rt2 * 256;
  const int oc = (lane & 7) ^ ((r5 >> 1) & 7);
  const unsigned voffO = (unsigned)(oc >> 2) * (2048 * 64) + (unsigned)r5 * 64 + (unsigned)(oc & 3) * 16;
  const char* gW0 = IS_A ? (const char*)W0 + (size_t)ct2 * 256 : (const char*)W0 + (size_t)ct2 * 128 * 2048;
  const char* gW1 = IS_A ? gW0 : (const char*)W1 + (size_t)ct2 * 128 * 2048;
  const int wBytes = IS_A ? 1024 * 2048 - ct2 * 256 : 128 * 2048;
  LAS char* dst0 = sm + lw * 1024;
  auto issAct = [&](int kt, int buf) {
    const rsrc_t r = mk_rsrc(gAct, kt < NT ? actBytes : 0);
    kt = (kt + rot) & (NT - 1);
#pragma unroll
    for (int i = 0; i < 4; ++i) {
      if constexpr (IS_A) dma16(r, dst0 + buf + i * 4096, voffO, (kt * 2 * 2048 + i * 32) * 64);
      else dma16(r, dst0 + buf + i * 4096, voffT, (kt * 64 + (i & 1) * 32) * 4096 + (i >> 1) * 128);
    }
  };
  auto issAct0 = [&]() {
    const rsrc_t r = mk_rsrc(gAct, actBytes);
    const int kt = rot & (NT - 1);
#pragma unroll
    for (int w = 0; w < 4; ++w) {
      const int ocw = (lane & 7) ^ ((4 * w + (lane >> 4)) & 7);
      const unsigned vo = (unsigned)(ocw >> 2) * (2048 * 64) + (unsigned)(8 * w + (lane >> 3)) * 64 + (unsigned)(ocw & 3) * 16;
#pragma unroll
      for (int i = 0; i < 4; ++i) {
        if constexpr (IS_A) dma16(r, sm + w * 1024 + i * 4096, vo, (kt * 2 * 2048 + i * 32) * 64);
        else dma16(r, sm + w * 1024 + i * 4096, voffTw(w), (kt * 64 + (i & 1) * 32) * 4096 + (i >> 1) * 128);
      }
    }
  };
  auto issW = [&](int kt, int buf) {
    const bool live = kt < NT;
    kt = (kt + rot) & (NT - 1);
    const rsrc_t r = mk_rsrc((IS_A || kt < 16) ? gW0 : gW1, live ? wBytes : 0);
#pragma unroll
    for (int i = 0; i < 4; ++i) {
      if constexpr (IS_A) dma16(r, dst0 + buf + 16384 + i * 4096, voffT, (kt * 64 + (i & 1) * 32) * 2048 + (i >> 1) * 128);
      else dma16(r, dst0 + buf + 16384 + i * 4096, voffR, (kt & 15) * 128 + i * 32 * 2048);
    }
  };
  STAMP(10);
#pragma unroll
  for (int s = 0; s < S - 1; ++s) issW(s, s * SB);
  STAMP(11);
  if (lw == 0) {
    poll_flag(flagp(ws, (IS_A ? FL_O : FL_H) + rt2 * 128 + (lane & 15) * 8), tag);
    STAMP(12);
    __builtin_amdgcn_fence(__ATOMIC_ACQUIRE, "agent");
    cfence();
    issAct0();
    wait_vm<16>();
    STAMP(13);
    if (upd_epoch && lane == 0) __hip_atomic_store(flagp(ws, IS_A ? FL_EPOCH_A : FL_EPOCH_B), tag, RLX_AGENT);
  }
  cfence(); bar(); cfence();
#pragma unroll
  for (int s = 1; s < S - 1; ++s) issAct(s, s * SB);
  STAMP(15);
  int wr = (S - 1) * SB;
#pragma unroll 1
  for (int t = 0; t < NT; ++t) {
    if (t < S - 2) { if (t == 0) wait_vm<4 * (S - 2)>(); else if (t == 1) wait_vm<4 * (S - 2) + 4>(); else wait_vm<4 * (S - 2) + 8>(); }
    else wait_vm<8 * (S - 2)>();
    bar(); cfence();
    issAct(t + S - 1, wr); issW(t + S - 1, wr);
    wr += SB; if (wr == S * SB) wr = 0;
  }
  wait_vm<0>();
  STAMP(16);
  STAMP_FLUSH2();
}

DI void publish_half(LAS unsigned* cnt, GAS u64* flag, u64 tag, int lane) {
  if (lane == 0) {
    const unsigned old = __hip_atomic_fetch_add(cnt, 1u, __ATOMIC_RELAXED, __HIP_MEMORY_SCOPE_WORKGROUP);
    if (old == 3u) __hip_atomic_store(flag, tag, RLX_AGENT);
  }
}

template <bool IS_A>
DI void phase2(LAS char* sm, char* ws, const u16* __restrict__ Bx, const u16* __restrict__ Dskip, float* __restrict__ out, int rt2, int ct2,
               int wave, int lane, unsigned (&g_st)[2]) {
  char* g_ws = ws;
  constexpr int NT = IS_A ? 16 : 32;
  constexpr int S = P2S, SB = 32768;
  const int wr_ = wave >> 2, wt = wave & 3;
  const int row0 = rt2 * 128 + (IS_A ? 64 * wr_ : 32 * wt) + (lane >> 3);
  const int col0 = ct2 * 128 + (IS_A ? 32 * wt : 64 * wr_) + 4 * (lane & 7);
  u32x2 xr[2][4], dr[2];
  if constexpr (!IS_A) {
#pragma unroll
    for (int j = 0; j < 2; ++j) {
      dr[j] = *(const u32x2*)(Dskip + col0 + 32 * j);
#pragma unroll
      for (int i = 0; i < 4; ++i) xr[j][i] = *(const u32x2*)(Bx + (size_t)(row0 + 8 * i) * 1024 + col0 + 32 * j);
    }
  }
  cfence(); bar(); cfence();
  STAMP(19);
  const int r16 = lane & 15, g = lane >> 4, q = r16 >> 2, p4 = lane & 3;
  int rofs[2], tofs[2];
#pragma unroll
  for (int s = 0; s < 2; ++s) {
    rofs[s] = (IS_A ? 0 : 16384) + (64 * wr_ + r16) * 128 + 16 * ((4 * s + g) ^ ((r16 >> 1) & 7));
    tofs[s] = (IS_A ? 16384 + (16 * (g & 1) + 4 * (g >> 1) + q) * 128 : (8 * g + q) * 128) + (wt >> 1) * 8192 + 8 * p4 +
              32 * ((2 * (wt & 1) + s) ^ (((q >> 1) & 1) | ((g & 1) << 1)));
  }
  f32x4 acc[4][2];
#pragma unroll
  for (int i = 0; i < 4; ++i)
#pragma unroll
    for (int j = 0; j < 2; ++j) acc[i][j] = (f32x4){0.f, 0.f, 0.f, 0.f};
  {
    bf16x8 fr[2][4];
    s16x4 tl[2][2][2];
#pragma unroll
    for (int i = 0; i < 4; ++i) { fr[1][i] = zero8(); tl[1][i >> 1][i & 1] = (s16x4){0, 0, 0, 0}; }
    auto mma = [&](auto B_, auto I) {
      constexpr int B = decltype(B_)::v, i = decltype(I)::v, ri = i >> 1, ti = i & 1;
      if constexpr (IS_A) acc[ri][ti] = MFMA16(cat8(tl[B][ti][0], tl[B][ti][1]), fr[B][ri], acc[ri][ti]);
      else acc[ri][ti] = MFMA16(fr[B][ri], cat8(tl[B][ti][0], tl[B][ti][1]), acc[ri][ti]);
    };
    auto rdf = [&](auto B_, auto S_, auto X, LAS char* sb) {
      constexpr int B = decltype(B_)::v, x = decltype(X)::v, s = decltype(S_)::v;
      constexpr int tt = IS_A ? 1024 : 512;
      if constexpr (x == 0) fr[B][0] = lds_b128(sb + rofs[s]);
      else if constexpr (x < 5) tl[B][(x - 1) >> 1][(x - 1) & 1] = lds_tr(sb + tofs[(x - 1) >> 1] + s * 4096 + ((x - 1) & 1) * tt);
      else fr[B][x - 4] = lds_b128(sb + rofs[s] + (x - 4) * 2048);
    };
    int rd = 0;
#pragma unroll 1
    for (int t = 0; t < NT; ++t) {
      wait_lgkm0(); bar(); cfence(); SCHED();
      LAS char* sb = sm + rd;
      mgroup<8, 8, P2RPM>([&](auto i) { mma(IC<1>{}, i); }, [&](auto x) { rdf(IC<0>{}, IC<0>{}, x, sb); });
      mgroup<8, 8, P2RPM>([&](auto i) { mma(IC<0>{}, i); }, [&](auto x) { rdf(IC<1>{}, IC<1>{}, x, sb); });
      rd += SB; if (rd == S * SB) rd = 0;
    }
    sfor<0, 8>([&](auto i) { mma(IC<1>{}, i); });
  }
  STAMP(20);
  {
    const rsrc_t ors = mk_rsrc(out, 2048 * 2048 * 4);
    const int obase = (row0 * 2048 + (IS_A ? 0 : 1024) + col0) * 4;
    LAS char* sc = sm + 131072 + wave * 4096;
    LAS char* wp = sc + r16 * 128;
    LAS char* rp = sc + (lane >> 3) * 128 + (((lane & 7) ^ (lane >> 3)) * 16);
#pragma unroll
    for (int j = 0; j < 2; ++j) {
#pragma unroll
      for (int e = 0; e < 4; ++e) {
        const int ri = 2 * j + (e >> 1), ti = e & 1;
        const int tt_ = IS_A ? (e >> 1) : ti, nt_ = IS_A ? ti : (e >> 1);
        *(LAS f32x4*)(wp + tt_ * 2048 + (((4 * nt_ + g) ^ (lane & 7)) * 16)) = acc[ri][ti];
      }
      wait_lgkm0();
      f32x4 v[4];
#pragma unroll
      for (int i = 0; i < 4; ++i) v[i] = *(LAS f32x4*)(rp + i * 1024);
#pragma unroll
      for (int i = 0; i < 4; ++i) {
        if constexpr (!IS_A) {
          v[i][0] = fmaf(bflo(xr[j][i][0]), bflo(dr[j][0]), v[i][0]); v[i][1] = fmaf(bfhi(xr[j][i][0]), bfhi(dr[j][0]), v[i][1]);
          v[i][2] = fmaf(bflo(xr[j][i][1]), bflo(dr[j][1]), v[i][2]); v[i][3] = fmaf(bfhi(xr[j][i][1]), bfhi(dr[j][1]), v[i][3]);
        }
        __builtin_amdgcn_raw_buffer_store_b128(__builtin_bit_cast(u32x4, v[i]), ors, obase + (IS_A ? j * 32 * 8192 : j * 128) + i * 8 * 8192, 0, OUT_AUX);
      }
      wait_lgkm0();
    }
  }
  STAMP(21);
}

__global__ __launch_bounds__(768, 3) void fused_kernel(
    const u16* __restrict__ Ax, const u16* __restrict__ Wqkv, const u16* __restrict__ Wphi, const u16* __restrict__ Wo,
    const u16* __restrict__ Bx, const u16* __restrict__ Lre, const u16* __restrict__ Lim, const u16* __restrict__ Bre,
    const u16* __restrict__ Bim, const u16* __restrict__ Cre, const u16* __restrict__ Cim, const u16* __restrict__ Logdt,
    const u16* __restrict__ Dskip, float* __restrict__ out, char* ws) {
  __shared__ __attribute__((aligned(1024))) char smem[LDS_BYTES];
  LAS char* sm = (LAS char*)smem;
  char* g_ws = ws;
  unsigned g_st[2] = {0u, 0u};
  STAMP(0);
  const int tid = threadIdx.x, lane = tid & 63, wave = __builtin_amdgcn_readfirstlane(tid >> 6);
  const int b = blockIdx.x, xcd = b & 7, idx = b >> 3, i16 = idx >> 1;
  const bool isA = (idx & 1) == 0;
  const int rt = 4 * (xcd & 1) + (i16 & 3), cg = 4 * (xcd >> 1) + (i16 >> 2);
  const int rt2 = 4 * (xcd & 3) + (i16 & 3), ct2 = 4 * (xcd >> 2) + (i16 >> 2);
  const int h = lane >> 5, c32 = lane & 31;
  const int rotx = 4 * (xcd >> 1) + 8 * (xcd & 1);
  const int rot1 = (isA ? ROTA : ROTB) == 9 ? rotx : (isA ? ROTA : ROTB) * xcd;
  constexpr int WR_OFF = 98304;
  constexpr int PH_PITCH = 136, PH_BYTES = 64 * PH_PITCH;
  constexpr int RED_OFF = 8 * PH_BYTES;
  constexpr int RED_W = 8192 + 256;
  constexpr int RED_GAP = 6912;
  constexpr int KVF_OFF = 147456;
  constexpr int CNT_OFF = 163824;
  constexpr int SC_OFF = 98304;
  constexpr int SB1 = 49152;
  constexpr int TE_OFF = 3 * SB1;
  constexpr int TG_OFF = TE_OFF + 4096;
  constexpr int TC_OFF = TG_OFF + 4096;

  if (wave >= 8) {
    const int lw = wave - 8;
    const int r5 = 8 * lw + (lane >> 3);
    const unsigned rowchunk = (unsigned)(((lane & 7) ^ ((r5 >> 1) & 7)) * 16);
    LAS char* dst0 = sm + lw * 1024;
    if (isA) {
      const unsigned voffA = (unsigned)r5 * 2048 + rowchunk;
      const unsigned voffB = (unsigned)r5 * 6144 + (unsigned)(((lane & 7) ^ (((r5 >> 1) & 1) << 2)) * 16);
      const char* gA = (const char*)Ax + (size_t)rt * 256 * 2048;
      const char* gB = (const char*)Wqkv + (size_t)cg * 128;
      auto issX = [&](int kt, int buf) {
        const rsrc_t r = mk_rsrc(gA, kt < 16 ? 256 * 2048 : 0);
#pragma unroll
        for (int i = 0; i < 8; ++i) dma16(r, dst0 + buf + i * 4096, voffA, ((kt + rot1) & 15) * 128 + i * 32 * 2048);
      };
      auto issW = [&](int j, int kt, int u) {
        const rsrc_t r = mk_rsrc(gB, kt < 16 ? 1024 * 6144 - cg * 128 : 0);
#pragma unroll
        for (int i = 0; i < 2; ++i) dma16(r, dst0 + WR_OFF + (u & 7) * 8192 + i * 4096, voffB, (((kt + rot1) & 15) * 64 + i * 32) * 6144 + j * 2048);
      };
      issX(0, 0); issW(0, 0, 0); issW(1, 0, 1); issW(2, 0, 2);
      u64 epoch = __hip_atomic_load(flagp(ws, FL_EPOCH_A), RLX_AGENT);
      u16 wraw[8];
#pragma unroll
      for (int j = 0; j < 8; ++j) wraw[j] = Wphi[(size_t)(2 * cg + (lw >> 1)) * 1024 + (16 * (lw & 1) + 8 * (j >> 2) + 4 * h + (j & 3)) * 32 + c32];
      cfence();
      issX(1, 32768); issW(0, 1, 3); issW(1, 1, 4);
      int xw = 65536, u0 = 0;
#pragma unroll 1
      for (int t = 0; t < 16; ++t) {
        wait_vm<12>(); bar(); cfence();
        issW(2, t + 1, u0 + 5); issX(t + 2, xw); issW(0, t + 2, u0 + 6); issW(1, t + 2, u0 + 7);
        xw += 32768; if (xw == 98304) xw = 0;
        u0 = (u0 + 3) & 7;
      }
      wait_vm<0>();
      {
        s16x8 w8;
#pragma unroll
        for (int j = 0; j < 8; ++j) w8[j] = (short)wraw[j];
        *(LAS s16x8*)(sm + WR_OFF + 2 * 8192 + lw * 1024 + lane * 16) = w8;
      }
      const u64 tag = epoch + 1ull;
      sync_lds();
      __builtin_amdgcn_s_setprio(3);
      bar(); cfence();
      if (tid == 512) *(LAS u32x4*)(sm + CNT_OFF) = (u32x4){0u, 0u, 0u, 0u};
      STAMP(3);
      {
        const rsrc_t kvrs = mk_rsrc(ws + WS_KVP + (size_t)(cg * 8 + rt) * KVP_BYTES, KVP_BYTES);
#pragma unroll
        for (int j = 0; j < 3; ++j) {
          if (j < 2 || lw == 0) {
            const int zl = lane & 15, zh = zl >> 3, zks = (zl >> 1) & 3, zhh = zl & 1;
            f32x4 s0 = {0.f, 0.f, 0.f, 0.f}, s1 = {0.f, 0.f, 0.f, 0.f};
#pragma unroll
            for (int w = 0; w < 4; ++w) {
              if (j < 2) {
                LAS char* rp = sm + RED_OFF + (2 * w + j) * RED_W + (2 * w + j >= 5 ? RED_GAP : 0) + (2 * lw) * 1024 + lane * 16;
                s0 += *(LAS f32x4*)rp; s1 += *(LAS f32x4*)(rp + 1024);
              } else {
                LAS char* rp = sm + RED_OFF + (2 * w + zh) * RED_W + (2 * w + zh >= 5 ? RED_GAP : 0) + 8192 + (((zks >> 1) * 2 + zhh) * 4 + 2 * (zks & 1)) * 16;
                s0 += *(LAS f32x4*)rp; s1 += *(LAS f32x4*)(rp + 16);
              }
            }
            const u32x4 d = {pk2(s0[0], s0[1]), pk2(s0[2], s0[3]), pk2(s1[0], s1[1]), pk2(s1[2], s1[3])};
            if (j < 2) __builtin_amdgcn_raw_buffer_store_b128(d, kvrs, ((j * 4 + lw) * 64 + lane) * 16, 0, 16);
            else if (lane < 16) __builtin_amdgcn_raw_buffer_store_b128(d, kvrs, 8192 + zl * 16, 0, 16);
          }
        }
        STAMP(4);
        wait_vm<0>();
        STAMP(5);
        if (lane == 0) __hip_atomic_store(flagp(ws, FL_KV + cg * 256 + rt * 32 + lw * 8), tag, RLX_AGENT);
        if (lw == 0) poll_flag(flagp(ws, FL_KV + cg * 256 + (lane & 31) * 8), tag);
      }
      STAMP(6);
      __builtin_amdgcn_s_setprio(0);
      sync_lds();
      bar(); cfence();
      phase2_load<true>(sm, ws, Wo, Wo, rt2, ct2, lw, lane, ROT2 * (4 * (xcd & 3) + 8 * (xcd >> 2)), tag, b == 0, g_st);
    } else {
      const int tloc = 32 * ((r5 >> 2) & 1) + 16 * (r5 & 1) + ((r5 & 3) >> 1) + 2 * (r5 >> 3);
      const unsigned voffA = (unsigned)tloc * 2048 + rowchunk;
      const unsigned voffB = (unsigned)r5 * 2048 + rowchunk;
      const char* gA = (const char*)Bx + (size_t)rt * 256 * 2048;
      const char* gBr = (const char*)Bre + (size_t)cg * 64 * 2048;
      const char* gBi = (const char*)Bim + (size_t)cg * 64 * 2048;
      auto issue = [&](int kt, int buf) {
        const bool live = kt < 16;
        kt = (kt + rot1) & 15;
        const rsrc_t ra = mk_rsrc(gA, live ? 256 * 2048 : 0), rr = mk_rsrc(gBr, live ? 64 * 2048 : 0), ri = mk_rsrc(gBi, live ? 64 * 2048 : 0);
#pragma unroll
        for (int i = 0; i < 8; ++i) dma16(ra, dst0 + buf + i * 4096, voffA, kt * 128 + ((i >> 1) * 64 + (i & 1) * 8) * 2048);
#pragma unroll
        for (int i = 8; i < 12; ++i) dma16(i < 10 ? rr : ri, dst0 + buf + i * 4096, voffB, kt * 128 + (i & 1) * 32 * 2048);
      };
      issue(0, 0);
      u64 epoch = __hip_atomic_load(flagp(ws, FL_EPOCH_B), RLX_AGENT);
      cfence();
      issue(1, SB1);
      int wr = 2 * SB1;
#pragma unroll 1
      for (int t = 0; t < 16; ++t) {
        wait_vm<12>(); bar(); cfence();
        issue(t + 2, wr);
        wr += SB1; if (wr == 3 * SB1) wr = 0;
      }
      wait_vm<0>();
      const u64 tag = epoch + 1ull;
      const unsigned tlo = (unsigned)tag, thi = (unsigned)(tag >> 32);
      STAMP(2);
      bar();
      if (tid == 512) *(LAS u32x4*)(sm + CNT_OFF) = (u32x4){0u, 0u, 0u, 0u};
      STAMP(3);
      {
        const rsrc_t agrs = mk_rsrc(ws + WS_AGG + (size_t)cg * 8192, rt * 1024);
        u32x4 gr[2];
#pragma unroll 1
        for (unsigned spins = 0; spins < SPIN_CAP; ++spins) {
          unsigned bad = 0;
#pragma unroll
          for (int k = 0; k < 2; ++k) gr[k] = __builtin_amdgcn_raw_buffer_load_b128(agrs, ((lw + 4 * k) * 64 + lane) * 16, 0, 16);
#pragma unroll
          for (int k = 0; k < 2; ++k) if (lw + 4 * k < rt) bad |= (gr[k][1] ^ tlo) | (gr[k][3] ^ thi);
          if (__all(bad == 0)) break;
          __builtin_amdgcn_s_sleep(POLL_SLEEP);
        }
#pragma unroll
        for (int k = 0; k < 2; ++k)
          *(LAS f32x2*)(sm + TG_OFF + ((lw + 4 * k) * 64 + lane) * 8) = (f32x2){__uint_as_float(gr[k][0]), __uint_as_float(gr[k][2])};
      }
      STAMP(4);
      sync_lds();
      phase2_load<false>(sm, ws, Cre, Cim, rt2, ct2, lw, lane, ROT2 * (8 * (xcd & 3) + 16 * (xcd >> 2)), tag, rt2 == 15 && ct2 == 0, g_st);
    }
    return;
  }

  const int wm = wave >> 1, wn = wave & 1;
  const int xs = (lane >> 1) & 7;
  const int q = (lane & 15) >> 2, p4 = lane & 3, blk = (lane >> 4) & 1;
  int rofs[4];
#pragma unroll
  for (int s = 0; s < 4; ++s) rofs[s] = c32 * 128 + 16 * ((2 * s + h) ^ xs);

  if (isA) {
    u64 epoch = __hip_atomic_load(flagp(ws, FL_EPOCH_A), RLX_AGENT);
    const int tofs = WR_OFF + (8 * h + q) * 128 + 64 * (wn ^ ((q >> 1) & 1)) + 32 * blk + 8 * p4;
    f32x16 Qt[2], Kt[2], V[2];
#pragma unroll
    for (int m = 0; m < 2; ++m)
#pragma unroll
      for (int e = 0; e < 16; ++e) { Qt[m][e] = 0.f; Kt[m][e] = 0.f; V[m][e] = 0.f; }
    STAMP(1);
    {
      bf16x8 fx[2][2];
      s16x4 fw[2][3][2];
#pragma unroll
      for (int i = 0; i < 2; ++i) fx[1][i] = zero8();
#pragma unroll
      for (int j = 0; j < 3; ++j) { fw[1][j][0] = (s16x4){0, 0, 0, 0}; fw[1][j][1] = (s16x4){0, 0, 0, 0}; }
      auto mma = [&](auto B_, auto I) {
        constexpr int B = decltype(B_)::v, i = decltype(I)::v, m = i / 3, j = i % 3;
        const bf16x8 w = cat8(fw[B][j][0], fw[B][j][1]);
        if constexpr (j == 0) Qt[m] = MFMA32(w, fx[B][m], Qt[m]);
        else if constexpr (j == 1) Kt[m] = MFMA32(w, fx[B][m], Kt[m]);
        else V[m] = MFMA32(fx[B][m], w, V[m]);
      };
      int xr = 0, u0 = 0;
#pragma unroll 1
      for (int t = 0; t < 16; ++t) {
        wait_lgkm0(); bar(); cfence(); SCHED();
        LAS char* xb = sm + xr + wm * 8192;
        LAS char* wb[3] = {sm + tofs + (u0 & 7) * 8192, sm + tofs + ((u0 + 1) & 7) * 8192, sm + tofs + ((u0 + 2) & 7) * 8192};
        auto rdf = [&](auto B_, auto S_, auto X) {
          constexpr int B = decltype(B_)::v, s = decltype(S_)::v, x = decltype(X)::v;
          if constexpr (x == 0) fx[B][0] = lds_b128(xb + rofs[s]);
          else if constexpr (x < 7) fw[B][(x - 1) >> 1][(x - 1) & 1] = lds_tr(wb[(x - 1) >> 1] + s * 2048 + ((x - 1) & 1) * 512);
          else fx[B][1] = lds_b128(xb + rofs[s] + 4096);
        };
        mgroup<6, 8, 2>([&](auto i) { mma(IC<1>{}, i); }, [&](auto x) { rdf(IC<0>{}, IC<0>{}, x); });
        mgroup<6, 8, 2>([&](auto i) { mma(IC<0>{}, i); }, [&](auto x) { rdf(IC<1>{}, IC<1>{}, x); });
        mgroup<6, 8, 2>([&](auto i) { mma(IC<1>{}, i); }, [&](auto x) { rdf(IC<0>{}, IC<2>{}, x); });
        mgroup<6, 8, 2>([&](auto i) { mma(IC<0>{}, i); }, [&](auto x) { rdf(IC<1>{}, IC<3>{}, x); });
        xr += 32768; if (xr == 98304) xr = 0;
        u0 = (u0 + 3) & 7;
      }
      sfor<0, 6>([&](auto i) { mma(IC<1>{}, i); });
    }
    asm volatile("" : "+v"(epoch));
    const u64 tag = epoch + 1ull;
    STAMP(2);
    sync_lds();
    STAMP(3);
    const bf16x8 wf0 = lds_b128(sm + WR_OFF + 2 * 8192 + (2 * wn) * 1024 + lane * 16);
    const bf16x8 wf1 = lds_b128(sm + WR_OFF + 2 * 8192 + (2 * wn + 1) * 1024 + lane * 16);
    f32x16 zero16;
#pragma unroll
    for (int e = 0; e < 16; ++e) zero16[e] = 0.f;
    LAS char* ph = sm + wave * PH_BYTES;
#pragma unroll
    for (int m = 0; m < 2; ++m) {
      f32x16 pk = MFMA32(wf0, pack8<0>(Kt[m]), zero16);
      pk = MFMA32(wf1, pack8<1>(Kt[m]), pk);
      f32x16 pp, pm;
      dual_exp<true>(pk, pp, pm);
      LAS char* rowp = ph + (32 * m + c32) * PH_PITCH + 8 * h;
#pragma unroll
      for (int g = 0; g < 4; ++g) {
        u32x2 a = {pk2(pp[4 * g], pp[4 * g + 1]), pk2(pp[4 * g + 2], pp[4 * g + 3])};
        u32x2 c = {pk2(pm[4 * g], pm[4 * g + 1]), pk2(pm[4 * g + 2], pm[4 * g + 3])};
        *(LAS u32x2*)(rowp + 16 * g) = a;
        *(LAS u32x2*)(rowp + 64 + 16 * g) = c;
      }
    }
    wait_lgkm0();
    STAMP(4);
    f32x16 KV[2], Z[2];
    KV[0] = zero16; KV[1] = zero16; Z[0] = zero16; Z[1] = zero16;
    {
      s16x8 o8;
#pragma unroll
      for (int j = 0; j < 8; ++j) o8[j] = (short)0x3f80;
      const bf16x8 ones = __builtin_bit_cast(bf16x8, o8);
      LAS char* tp = ph + (4 * h + q) * PH_PITCH + 32 * blk + 8 * p4;
#pragma unroll
      for (int m = 0; m < 2; ++m) {
#pragma unroll
        for (int s = 0; s < 2; ++s) {
          const bf16x8 vb = s ? pack8<1>(V[m]) : pack8<0>(V[m]);
          LAS char* t0 = tp + (32 * m + 16 * s) * PH_PITCH;
#pragma unroll
          for (int fs = 0; fs < 2; ++fs) {
            const bf16x8 af = cat8(lds_tr(t0 + 64 * fs), lds_tr(t0 + 8 * PH_PITCH + 64 * fs));
            KV[fs] = MFMA32(af, vb, KV[fs]);
            Z[fs] = MFMA32(af, ones, Z[fs]);
          }
        }
      }
    }
    {
      LAS char* rw = sm + RED_OFF + wave * RED_W + (wave >= 5 ? RED_GAP : 0);
#pragma unroll
      for (int fs = 0; fs < 2; ++fs)
#pragma unroll
        for (int g = 0; g < 4; ++g) {
          f32x4 v = {KV[fs][4 * g], KV[fs][4 * g + 1], KV[fs][4 * g + 2], KV[fs][4 * g + 3]};
          *(LAS f32x4*)(rw + (fs * 4 + g) * 1024 + lane * 16) = v;
        }
      if (c32 == 0) {
#pragma unroll
        for (int fs = 0; fs < 2; ++fs)
#pragma unroll
          for (int g = 0; g < 4; ++g) {
            f32x4 z = {Z[fs][4 * g], Z[fs][4 * g + 1], Z[fs][4 * g + 2], Z[fs][4 * g + 3]};
            *(LAS f32x4*)(rw + 8192 + ((fs * 2 + h) * 4 + g) * 16) = z;
          }
      }
    }
    STAMP(5);
    sync_lds();
    STAMP(6);
    STAMP(9);
    bf16x8 fq[2][4];
#pragma unroll
    for (int m = 0; m < 2; ++m) {
      f32x16 pq = MFMA32(wf0, pack8<0>(Qt[m]), zero16);
      pq = MFMA32(wf1, pack8<1>(Qt[m]), pq);
      f32x16 pp, pm;
      dual_exp<false>(pq, pp, pm);
      fq[m][0] = pack8<0>(pp); fq[m][1] = pack8<1>(pp); fq[m][2] = pack8<0>(pm); fq[m][3] = pack8<1>(pm);
    }
    asm volatile("" : "+v"(fq[0][0]), "+v"(fq[0][1]), "+v"(fq[0][2]), "+v"(fq[0][3]), "+v"(fq[1][0]), "+v"(fq[1][1]), "+v"(fq[1][2]), "+v"(fq[1][3]));
    STAMP(10);
    cfence(); bar(); cfence();
    STAMP(12);
    {
      const rsrc_t kvrs = mk_rsrc(ws + WS_KVP + (size_t)cg * 8 * KVP_BYTES, 8 * KVP_BYTES);
      u32x4 v[8], vz[8];
#pragma unroll
      for (int r = 0; r < 8; ++r) v[r] = __builtin_amdgcn_raw_buffer_load_b128(kvrs, tid * 16, r * KVP_BYTES, 16);
      if (wave == 7) {
#pragma unroll
        for (int r = 0; r < 8; ++r) vz[r] = __builtin_amdgcn_raw_buffer_load_b128(kvrs, 8192 + (lane & 15) * 16, r * KVP_BYTES, 16);
      }
      auto sum8 = [&](const u32x4 (&x)[8]) {
        f32x4 a = {0.f, 0.f, 0.f, 0.f}, c = {0.f, 0.f, 0.f, 0.f};
#pragma unroll
        for (int r = 0; r < 8; ++r) {
          a += (f32x4){bflo(x[r][0]), bfhi(x[r][0]), bflo(x[r][1]), bfhi(x[r][1])};
          c += (f32x4){bflo(x[r][2]), bfhi(x[r][2]), bflo(x[r][3]), bfhi(x[r][3])};
        }
        return (u32x4){pk2(a[0], a[1]), pk2(a[2], a[3]), pk2(c[0], c[1]), pk2(c[2], c[3])};
      };
      *(LAS u32x4*)(sm + KVF_OFF + tid * 16) = sum8(v);
      if (wave == 7) { const u32x4 z = sum8(vz); if (lane < 16) *(LAS u32x4*)(sm + KVF_OFF + 8192 + lane * 16) = z; }
    }
    STAMP(13);
    sync_lds();
    STAMP(14);
    {
      bf16x8 kf[4], zf[4];
      s16x8 o8;
#pragma unroll
      for (int j = 0; j < 8; ++j) o8[j] = (short)0x3f80;
#pragma unroll
      for (int ks = 0; ks < 4; ++ks) {
        kf[ks] = lds_b128(sm + KVF_OFF + wn * 4096 + ks * 1024 + lane * 16);
        const bf16x8 z = lds_b128(sm + KVF_OFF + 8192 + wn * 128 + (ks * 2 + h) * 16);
        zf[ks] = (lane & 1) ? __builtin_bit_cast(bf16x8, o8) : z;
      }
      LAS char* sc = sm + SC_OFF + wave * 5120;
#pragma unroll
      for (int m = 0; m < 2; ++m) {
        f32x16 np = MFMA32(kf[0], fq[m][0], zero16), nm = MFMA32(kf[2], fq[m][2], zero16);
        f32x16 dp = MFMA32(zf[0], fq[m][0], zero16), dm = MFMA32(zf[2], fq[m][2], zero16);
        np = MFMA32(kf[1], fq[m][1], np); nm = MFMA32(kf[3], fq[m][3], nm);
        dp = MFMA32(zf[1], fq[m][1], dp); dm = MFMA32(zf[3], fq[m][3], dm);
        const float rp = __builtin_amdgcn_rcpf(dp[1]), rm = __builtin_amdgcn_rcpf(dm[1]);
        const float rinv = 1.0f / (dp[0] * rp + dm[0] * rm + 1e-6f);
        const float cp = rp * rinv, cm = rm * rinv;
        f32x16 o;
#pragma unroll
        for (int e = 0; e < 16; ++e) o[e] = np[e] * cp + nm[e] * cm;
        LAS char* op = sc + (32 * m + c32) * 80 + h * 32;
        *(LAS bf16x8*)op = pack8<0>(o);
        *(LAS bf16x8*)(op + 16) = pack8<1>(o);
      }
      wait_lgkm0();
      const rsrc_t ors = mk_rsrc(ws + WS_O, 2048 * 2048);
      const int obase = ((2 * cg + wn) * 2048 + rt * 256 + 64 * wm) * 64 + lane * 16;
#pragma unroll
      for (int j = 0; j < 4; ++j) {
        const u32x4 v = *(LAS u32x4*)(sc + (16 * j + (lane >> 2)) * 80 + (lane & 3) * 16);
        __builtin_amdgcn_raw_buffer_store_b128(v, ors, obase + j * 1024, 0, 16);
      }
    }
    STAMP(15);
    wait_vm<0>();
    STAMP(16);
    publish_half((LAS unsigned*)(sm + CNT_OFF) + (wave >> 2), flagp(ws, FL_O + (2 * rt + (wave >> 2)) * 128 + cg * 8), tag, lane);
    phase2<true>(sm, ws, Bx, Dskip, out, rt2, ct2, wave, lane, g_st);
  } else {
    u64 epoch = __hip_atomic_load(flagp(ws, FL_EPOCH_B), RLX_AGENT);
    const uintptr_t pw0 = (uintptr_t)Wqkv, pw1 = (uintptr_t)Cre, pw2 = (uintptr_t)Cim, pw3 = (uintptr_t)Wo;
    if (wave == 7) {
      const int ch = 64 * cg + lane;
      const float lamr = bf2f(Lre[ch]), lami = bf2f(Lim[ch]);
      const float dt = expf(bf2f(Logdt[ch]));
      const float zr = dt * lamr, zi = dt * lami;
      float sn, cs;
      sincosf(zi, &sn, &cs);
      const float mag = expf(zr);
      const float lr = mag * cs, li = mag * sn;
      const float nr = lr - 1.0f, ni = li, dd = 2.0f / (lamr * lamr + lami * lami);
      const float c2r = (nr * lamr + ni * lami) * dd, c2i = (ni * lamr - nr * lami) * dd;
      float pr = lr, pi = li;
#pragma unroll
      for (int i = 0; i < 4; ++i) { const float t = pr * pr - pi * pi; pi = 2.0f * pr * pi; pr = t; }
      const float l16r = pr, l16i = pi;
      { const float t = pr * pr - pi * pi; pi = 2.0f * pr * pi; pr = t; }
      const float l32r = pr, l32i = pi;
#pragma unroll
      for (int i = 0; i < 3; ++i) { const float t = pr * pr - pi * pi; pi = 2.0f * pr * pi; pr = t; }
      LAS f32x4* tc = (LAS f32x4*)(sm + TC_OFF + lane * 48);
      tc[0] = (f32x4){lr, li, c2r, c2i}; tc[1] = (f32x4){l16r, l16i, l32r, l32i}; tc[2] = (f32x4){pr, pi, 0.f, 0.f};
    }
    f32x16 Ur[2], Ui[2];
#pragma unroll
    for (int m = 0; m < 2; ++m)
#pragma unroll
      for (int e = 0; e < 16; ++e) { Ur[m][e] = 0.f; Ui[m][e] = 0.f; }
    STAMP(1);
    {
      bf16x8 fx[2][2], fb[2][2];
#pragma unroll
      for (int i = 0; i < 2; ++i) { fx[1][i] = zero8(); fb[1][i] = zero8(); }
      auto mma = [&](auto B_, auto I) {
        constexpr int B = decltype(B_)::v, i = decltype(I)::v;
        if constexpr (i == 0) Ur[0] = MFMA32(fx[B][0], fb[B][0], Ur[0]);
        else if constexpr (i == 1) Ui[0] = MFMA32(fx[B][0], fb[B][1], Ui[0]);
        else if constexpr (i == 2) Ur[1] = MFMA32(fx[B][1], fb[B][0], Ur[1]);
        else Ui[1] = MFMA32(fx[B][1], fb[B][1], Ui[1]);
      };
      int rd = 0;
#pragma unroll 1
      for (int t = 0; t < 16; ++t) {
        wait_lgkm0(); bar(); cfence(); SCHED();
        if (WARM_ON && (t & 7) == wave && t < 12 && (t >= 6 || WARM_ON > 1)) {
          const uintptr_t wb_ = t < 6 ? pw0 : (t < 8 ? pw1 : (t < 10 ? pw2 : pw3));
          l2touch(mk_rsrc((const void*)wb_, 0x7fffffff), (unsigned)lane * 128, ((t < 6 ? t : (t & 1)) * 128 + xcd * 16 + i16) * 8192);
        }
        LAS char* xb = sm + rd + wm * 8192;
        LAS char* bb = sm + rd + 32768 + wn * 4096;
        auto rdf = [&](auto B_, auto S_, auto X) {
          constexpr int B = decltype(B_)::v, s = decltype(S_)::v, x = decltype(X)::v;
          if constexpr (x == 0) fx[B][0] = lds_b128(xb + rofs[s]);
          else if constexpr (x == 1) fb[B][0] = lds_b128(bb + rofs[s]);
          else if constexpr (x == 2) fb[B][1] = lds_b128(bb + rofs[s] + 8192);
          else fx[B][1] = lds_b128(xb + rofs[s] + 4096);
        };
        mgroup<4, 4, 2>([&](auto i) { mma(IC<1>{}, i); }, [&](auto x) { rdf(IC<0>{}, IC<0>{}, x); });
        mgroup<4, 4, 2>([&](auto i) { mma(IC<0>{}, i); }, [&](auto x) { rdf(IC<1>{}, IC<1>{}, x); });
        mgroup<4, 4, 2>([&](auto i) { mma(IC<1>{}, i); }, [&](auto x) { rdf(IC<0>{}, IC<2>{}, x); });
        mgroup<4, 4, 2>([&](auto i) { mma(IC<0>{}, i); }, [&](auto x) { rdf(IC<1>{}, IC<3>{}, x); });
        rd += SB1; if (rd == 3 * SB1) rd = 0;
      }
      sfor<0, 4>([&](auto i) { mma(IC<1>{}, i); });
    }
    asm volatile("" : "+v"(epoch));
    const u64 tag = epoch + 1ull;
    const unsigned tlo = (unsigned)tag, thi = (unsigned)(tag >> 32);
    STAMP(2);
    const int seg = 2 * wm + h, chl = 32 * wn + c32;
    const f32x4 k0 = *(LAS f32x4*)(sm + TC_OFF + chl * 48), k1 = *(LAS f32x4*)(sm + TC_OFF + chl * 48 + 16), k2 = *(LAS f32x4*)(sm + TC_OFF + chl * 48 + 32);
    const f32x2 lr2 = {k0[0], k0[0]}, li2 = {k0[1], k0[1]}, cr2 = {k0[2], k0[2]}, ci2 = {k0[3], k0[3]};
    const float l16r = k1[0], l16i = k1[1], l32r = k1[2], l32i = k1[3], l256r = k2[0], l256i = k2[1];
    f32x2 er = {0.f, 0.f}, ei = {0.f, 0.f};
#pragma unroll
    for (int m = 0; m < 2; ++m)
#pragma unroll
      for (int i = 0; i < 16; i += 2) {
        const f32x2 ur = {Ur[m][i], Ur[m][i + 1]}, ui = {Ui[m][i], Ui[m][i + 1]};
        const f32x2 nr = __builtin_elementwise_fma(lr2, er, __builtin_elementwise_fma(-li2, ei, ur));
        const f32x2 ni = __builtin_elementwise_fma(lr2, ei, __builtin_elementwise_fma(li2, er, ui));
        er = nr; ei = ni;
      }
    const float e0r = er[0], e0i = ei[0];
    *(LAS f32x2*)(sm + TE_OFF + (seg * 64 + chl) * 8) =
        (f32x2){fmaf(l16r, e0r, fmaf(-l16i, e0i, er[1])), fmaf(l16r, e0i, fmaf(l16i, e0r, ei[1]))};
    STAMP(3);
    sync_lds();
    STAMP(7);
    f32x2 E[8];
#pragma unroll
    for (int s = 0; s < 8; ++s) E[s] = *(LAS f32x2*)(sm + TE_OFF + (s * 64 + chl) * 8);
    const rsrc_t agrs = mk_rsrc(ws + WS_AGG + (size_t)cg * 8192, 8192);
    if (wm == 0) {
      float tr = 0.f, ti = 0.f;
#pragma unroll
      for (int s = 0; s < 8; ++s) { const float nr = fmaf(l32r, tr, fmaf(-l32i, ti, E[s][0])), ni = fmaf(l32r, ti, fmaf(l32i, tr, E[s][1])); tr = nr; ti = ni; }
      if (h == 0) {
        const u32x4 gr = {__float_as_uint(tr), tlo, __float_as_uint(ti), thi};
        __builtin_amdgcn_raw_buffer_store_b128(gr, agrs, (rt * 64 + chl) * 16, 0, 16);
      }
    }
    STAMP(4);
    sync_lds();
    STAMP(6);
    float sr = 0.f, si = 0.f;
#pragma unroll 1
    for (int r = 0; r < rt; ++r) {
      const f32x2 g = *(LAS f32x2*)(sm + TG_OFF + (r * 64 + chl) * 8);
      const float nr = fmaf(l256r, sr, fmaf(-l256i, si, g[0])), ni = fmaf(l256r, si, fmaf(l256i, sr, g[1]));
      sr = nr; si = ni;
    }
#pragma unroll
    for (int s = 0; s < 7; ++s) {
      const float nr = fmaf(l32r, sr, fmaf(-l32i, si, E[s][0])), ni = fmaf(l32r, si, fmaf(l32i, sr, E[s][1]));
      if (s < seg) { sr = nr; si = ni; }
    }
    er = (f32x2){sr, fmaf(l16r, sr, fmaf(-l16i, si, e0r))};
    ei = (f32x2){si, fmaf(l16r, si, fmaf(l16i, sr, e0i))};
    u32x4 wre[2][2], wim[2][2];
#pragma unroll
    for (int m = 0; m < 2; ++m) {
      f32x2 hr[8], hi[8];
#pragma unroll
      for (int i = 0; i < 16; i += 2) {
        const f32x2 ur = {Ur[m][i], Ur[m][i + 1]}, ui = {Ui[m][i], Ui[m][i + 1]};
        const f32x2 nr = __builtin_elementwise_fma(lr2, er, __builtin_elementwise_fma(-li2, ei, ur));
        const f32x2 ni = __builtin_elementwise_fma(lr2, ei, __builtin_elementwise_fma(li2, er, ui));
        er = nr; ei = ni;
        hr[i >> 1] = __builtin_elementwise_fma(cr2, er, -(ci2 * ei));
        hi[i >> 1] = -__builtin_elementwise_fma(cr2, ei, ci2 * er);
      }
#pragma unroll
      for (int c = 0; c < 2; ++c) {
        wre[c][m] = (u32x4){pk2(hr[0][c], hr[1][c]), pk2(hr[2][c], hr[3][c]), pk2(hr[4][c], hr[5][c]), pk2(hr[6][c], hr[7][c])};
        wim[c][m] = (u32x4){pk2(hi[0][c], hi[1][c]), pk2(hi[2][c], hi[3][c]), pk2(hi[4][c], hi[5][c]), pk2(hi[6][c], hi[7][c])};
      }
    }
    STAMP(5);
    {
      const rsrc_t hrs = mk_rsrc(ws + WS_H, 2048 * 4096);
      LAS char* sc = sm + SC_OFF + wave * 5120;
      LAS char* wp = sc + c32 * 144 + 64 * h;
      LAS char* rp = sc + (lane >> 3) * 144 + (lane & 7) * 16;
      const int hbase = (cg * 64 + 32 * wn + (lane >> 3)) * 4096 + rt * 512 + wm * 128 + (lane & 7) * 16;
      const int imo = (32768 + (wave >> 2) * 32768 + (wave & 3) * 4096) - (SC_OFF + wave * 5120);
      LAS char* wp2 = sc + imo + c32 * 128; LAS char* rp2 = sc + imo + (lane >> 3) * 128 + (((lane & 7) ^ (lane >> 3)) * 16);
#pragma unroll
      for (int c = 0; c < 2; ++c)
#pragma unroll
        for (int m = 0; m < 2; ++m) {
          *(LAS u32x4*)(wp + 32 * c + 16 * m) = wre[c][m];
          *(LAS u32x4*)(wp2 + (((4 * h + 2 * c + m) ^ (lane & 7)) * 16)) = wim[c][m];
        }
      wait_lgkm0();
      u32x4 v[8];
#pragma unroll
      for (int j = 0; j < 4; ++j) { v[j] = *(LAS u32x4*)(rp + j * 8 * 144); v[4 + j] = *(LAS u32x4*)(rp2 + j * 1024); }
#pragma unroll
      for (int j = 0; j < 8; ++j) __builtin_amdgcn_raw_buffer_store_b128(v[j], hrs, hbase + ((j >> 2) * 1024 + 8 * (j & 3)) * 4096, 0, 16);
    }
    STAMP(15);
    wait_vm<0>();
    STAMP(16);
    publish_half((LAS unsigned*)(sm + CNT_OFF) + (wave >> 2), flagp(ws, FL_H + (2 * rt + (wave >> 2)) * 128 + cg * 8), tag, lane);
    phase2<false>(sm, ws, Bx, Dskip, out, rt2, ct2, wave, lane, g_st);
  }
}

extern "C" void kernel_launch(void* const* d_in, const int* in_sizes, int n_in, void* d_out, int out_size, void* d_ws,
                              size_t ws_size, hipStream_t stream) {
  fused_kernel<<<256, 768, 0, stream>>>((const u16*)d_in[0], (const u16*)d_in[1], (const u16*)d_in[2], (const u16*)d_in[3],
                                        (const u16*)d_in[4], (const u16*)d_in[5], (const u16*)d_in[6], (const u16*)d_in[7],
                                        (const u16*)d_in[8], (const u16*)d_in[9], (const u16*)d_in[10], (const u16*)d_in[11],
                                        (const u16*)d_in[12], (float*)d_out, (char*)d_ws);
}
```

```cpp
#include <hip/hip_runtime.h>
#include <stdint.h>

#define LAS __attribute__((address_space(3)))
#define GAS __attribute__((address_space(1)))
#define DI __device__ __forceinline__

typedef unsigned short u16;
typedef unsigned long long u64;
typedef __bf16 bf16x8 __attribute__((ext_vector_type(8)));
typedef __bf16 bf16x2 __attribute__((ext_vector_type(2)));
typedef short s16x4 __attribute__((ext_vector_type(4)));
typedef short s16x8 __attribute__((ext_vector_type(8)));
typedef float f32x2 __attribute__((ext_vector_type(2)));
typedef float f32x4 __attribute__((ext_vector_type(4)));
typedef unsigned u32x2 __attribute__((ext_vector_type(2)));
typedef unsigned u32x4 __attribute__((ext_vector_type(4)));

#define MFMA16(a, b, c) __builtin_amdgcn_mfma_f32_16x16x32_bf16((a), (b), (c), 0, 0, 0)
#define RLX_AGENT __ATOMIC_RELAXED, __HIP_MEMORY_SCOPE_AGENT

constexpr size_t WS_O    = 0;
constexpr size_t WS_H    = 4u << 20;
constexpr size_t WS_KVP  = 12u << 20;
constexpr int    KVP_BYTES = 8448;
constexpr size_t WS_AGG  = 15u << 20;
constexpr size_t WS_FLAG = 16u << 20;
constexpr int FL_EPOCH_A = 0, FL_EPOCH_B = 256;
constexpr int FL_KV = 4096;
constexpr int FL_O = 32768;
constexpr int FL_H = 65536;
constexpr int LDS_BYTES = 160 * 1024;
constexpr unsigned SPIN_CAP = 1u << 20;
constexpr int POLL_SLEEP = 2;
constexpr int OUT_AUX = 2;
constexpr int P2S = 4;

#define STAMP_FLUSH2() do {} while (0)
#define STAMP(i) do {} while (0)
#define STAMP_FLUSH() do {} while (0)

DI float bf2f(u16 v) { return __uint_as_float((unsigned)v << 16); }
DI float bflo(unsigned v) { return __uint_as_float(v << 16); }
DI float bfhi(unsigned v) { return __uint_as_float(v & 0xffff0000u); }
DI unsigned pk2(float lo, float hi) { f32x2 x = {lo, hi}; return __builtin_bit_cast(unsigned, __builtin_convertvector(x, bf16x2)); }
template <int N> DI void wait_vm() { asm volatile("s_waitcnt vmcnt(%0)" ::"i"(N) : "memory"); }
DI void wait_lgkm0() { asm volatile("s_waitcnt lgkmcnt(0)" ::: "memory"); }
DI void bar() { __builtin_amdgcn_s_barrier(); }
DI void cfence() { asm volatile("" ::: "memory"); }
DI void sync_lds() { wait_lgkm0(); bar(); cfence(); }
DI bf16x8 lds_b128(LAS char* p) { return *(LAS bf16x8*)p; }
DI s16x4 lds_tr(LAS char* p) { return __builtin_amdgcn_ds_read_tr16_b64_v4i16((LAS s16x4*)p); }
DI bf16x8 cat8(s16x4 lo, s16x4 hi) { return __builtin_bit_cast(bf16x8, __builtin_shufflevector(lo, hi, 0, 1, 2, 3, 4, 5, 6, 7)); }
DI bf16x8 zero8() { s16x8 z = {0, 0, 0, 0, 0, 0, 0, 0}; return __builtin_bit_cast(bf16x8, z); }
DI GAS u64* flagp(char* ws, int off) { return (GAS u64*)(ws + WS_FLAG + off); }
DI float vmax(float a, float b) { return __builtin_elementwise_maximum(a, b); }
DI float vmin(float a, float b) { return __builtin_elementwise_minimum(a, b); }

#define SCHED() __builtin_amdgcn_sched_barrier(0)
template <int I> struct IC { static constexpr int v = I; };
template <int B, int E, class F> DI void sfor(F f) { if constexpr (B < E) { f(IC<B>{}); sfor<B + 1, E>(f); } }
typedef __amdgpu_buffer_rsrc_t rsrc_t;
DI rsrc_t mk_rsrc(const void* p, int bytes) { return __builtin_amdgcn_make_buffer_rsrc((void*)p, 0, bytes, 0x00020000); }
DI void dma16(rsrc_t r, LAS char* l, unsigned voff, int soff) {
  asm volatile("s_mov_b32 m0, %0\n\ts_nop 0\n\tbuffer_load_dwordx4 %1, %2, %3 offen lds"
               ::"s"((unsigned)(size_t)l), "v"(voff), "s"(r), "s"(soff) : "memory");
}
DI void l2touch(rsrc_t r, unsigned voff, int soff) {
  asm volatile("buffer_load_dword v167, %0, %1, %2 offen" ::"v"(voff), "s"(r), "s"(soff) : "memory", "v167");
}
template <int NM, int NR, int RPM, class M, class R>
DI void mgroup(M mma, R rd) {
  sfor<0, NM>([&](auto i) {
    constexpr int I = decltype(i)::v;
    mma(i); SCHED();
    sfor<0, RPM>([&](auto j) { constexpr int X = I * RPM + decltype(j)::v; if constexpr (X < NR) rd(IC<X>{}); });
    SCHED();
  });
}

DI bf16x8 pack44(const f32x4& a, const f32x4& b) {
  return __builtin_bit_cast(bf16x8, (u32x4){pk2(a[0], a[1]), pk2(a[2], a[3]), pk2(b[0], b[1]), pk2(b[2], b[3])});
}
template <bool MAX> DI void red2(float& a, float& b) {
  auto r = __builtin_amdgcn_permlane32_swap(__float_as_uint(a), __float_as_uint(b), false, false);
  float c = MAX ? vmax(__uint_as_float(r[0]), __uint_as_float(r[1])) : __uint_as_float(r[0]) + __uint_as_float(r[1]);
  auto r2 = __builtin_amdgcn_permlane16_swap(__float_as_uint(c), __float_as_uint(c), false, false);
  c = MAX ? vmax(__uint_as_float(r2[0]), __uint_as_float(r2[1])) : __uint_as_float(r2[0]) + __uint_as_float(r2[1]);
  auto r3 = __builtin_amdgcn_permlane32_swap(__float_as_uint(c), __float_as_uint(c), false, false);
  a = __uint_as_float(r3[0]); b = __uint_as_float(r3[1]);
}
template <bool NORM> DI void dual_exp(const f32x4 (&x)[2], f32x4 (&pp)[2], f32x4 (&pm)[2]) {
  float mx = vmax(vmax(vmax(x[0][0], x[0][1]), x[0][2]), vmax(vmax(x[0][3], x[1][0]), x[1][1]));
  float mn = vmin(vmin(vmin(x[0][0], x[0][1]), x[0][2]), vmin(vmin(x[0][3], x[1][0]), x[1][1]));
  mx = vmax(vmax(mx, x[1][2]), x[1][3]); mn = -vmin(vmin(mn, x[1][2]), x[1][3]);
  red2<true>(mx, mn);
  const float L2E = 1.4426950408889634f;
  const float nmx = -mx * L2E, nmn = -mn * L2E;
  float sp = 0.f, sm = 0.f;
#pragma unroll
  for (int f = 0; f < 2; ++f)
#pragma unroll
    for (int i = 0; i < 4; ++i) {
      pp[f][i] = __builtin_amdgcn_exp2f(fmaf(x[f][i], L2E, nmx));
      pm[f][i] = __builtin_amdgcn_exp2f(fmaf(x[f][i], -L2E, nmn));
      if constexpr (NORM) { sp += pp[f][i]; sm += pm[f][i]; }
    }
  if constexpr (NORM) {
    red2<false>(sp, sm);
    const float rp = __builtin_amdgcn_rcpf(sp), rm = __builtin_amdgcn_rcpf(sm);
#pragma unroll
    for (int f = 0; f < 2; ++f) { pp[f] *= rp; pm[f] *= rm; }
  }
}

DI void poll_flag(GAS u64* p, u64 tag) {
#pragma unroll 1
  for (unsigned spins = 0; spins < SPIN_CAP; ++spins) {
    const u64 v = __hip_atomic_load(p, RLX_AGENT);
    if (__all(v == tag)) break;
    __builtin_amdgcn_s_sleep(POLL_SLEEP);
  }
}

template <bool IS_A>
DI void phase2_load(LAS char* sm, char* ws, const u16* __restrict__ W0, const u16* __restrict__ W1, int rt2, int ct2, int lw, int lane,
                    int rot, u64 tag, bool upd_epoch, unsigned (&g_st)[2]) {
  constexpr int NT = IS_A ? 16 : 32;
  constexpr int S = P2S, SB = 32768;
  const int r5 = 8 * lw + (lane >> 3);
  const unsigned voffR = (unsigned)r5 * 2048 + (unsigned)(((lane & 7) ^ ((r5 >> 1) & 7)) * 16);
  auto voffTw = [&](int w) {
    const int k = 8 * w + (lane >> 3), f2 = ((k >> 1) & 1) | (((k >> (IS_A ? 2 : 3)) & 1) << 1);
    return (unsigned)k * (IS_A ? 2048 : 4096) + (unsigned)(((lane & 7) ^ (f2 << 1)) * 16);
  };
  const unsigned voffT = voffTw(lw);
  const char* gAct = IS_A ? ws + WS_O + (size_t)rt2 * 128 * 64 : ws + WS_H + (size_t)rt2 * 256;
  const int actBytes = IS_A ? 2048 * 2048 - rt2 * 128 * 64 : 2048 * 4096 - rt2 * 256;
  const int oc = (lane & 7) ^ ((r5 >> 1) & 7);
  const unsigned voffO = (unsigned)(oc >> 2) * (2048 * 64) + (unsigned)r5 * 64 + (unsigned)(oc & 3) * 16;
  const char* gW0 = IS_A ? (const char*)W0 + (size_t)ct2 * 256 : (const char*)W0 + (size_t)ct2 * 128 * 2048;
  const char* gW1 = IS_A ? gW0 : (const char*)W1 + (size_t)ct2 * 128 * 2048;
  const int wBytes = IS_A ? 1024 * 2048 - ct2 * 256 : 128 * 2048;
  LAS char* dst0 = sm + lw * 1024;
  auto issAct = [&](int kt, int buf) {
    const rsrc_t r = mk_rsrc(gAct, kt < NT ? actBytes : 0);
    kt = (kt + rot) & (NT - 1);
#pragma unroll
    for (int i = 0; i < 4; ++i) {
      if constexpr (IS_A) dma16(r, dst0 + buf + i * 4096, voffO, (kt * 2 * 2048 + i * 32) * 64);
      else dma16(r, dst0 + buf + i * 4096, voffT, (kt * 64 + (i & 1) * 32) * 4096 + (i >> 1) * 128);
    }
  };
  auto issAct0 = [&]() {
    const rsrc_t r = mk_rsrc(gAct, actBytes);
    const int kt = rot & (NT - 1);
#pragma unroll
    for (int w = 0; w < 4; ++w) {
      const int ocw = (lane & 7) ^ ((4 * w + (lane >> 4)) & 7);
      const unsigned vo = (unsigned)(ocw >> 2) * (2048 * 64) + (unsigned)(8 * w + (lane >> 3)) * 64 + (unsigned)(ocw & 3) * 16;
#pragma unroll
      for (int i = 0; i < 4; ++i) {
        if constexpr (IS_A) dma16(r, sm + w * 1024 + i * 4096, vo, (kt * 2 * 2048 + i * 32) * 64);
        else dma16(r, sm + w * 1024 + i * 4096, voffTw(w), (kt * 64 + (i & 1) * 32) * 4096 + (i >> 1) * 128);
      }
    }
  };
  auto issW = [&](int kt, int buf) {
    const bool live = kt < NT;
    kt = (kt + rot) & (NT - 1);
    const rsrc_t r = mk_rsrc((IS_A || kt < 16) ? gW0 : gW1, live ? wBytes : 0);
#pragma unroll
    for (int i = 0; i < 4; ++i) {
      if constexpr (IS_A) dma16(r, dst0 + buf + 16384 + i * 4096, voffT, (kt * 64 + (i & 1) * 32) * 2048 + (i >> 1) * 128);
      else dma16(r, dst0 + buf + 16384 + i * 4096, voffR, (kt & 15) * 128 + i * 32 * 2048);
    }
  };
  STAMP(10);
#pragma unroll
  for (int s = 0; s < S - 1; ++s) issW(s, s * SB);
  STAMP(11);
  if (lw == 0) {
    poll_flag(flagp(ws, (IS_A ? FL_O : FL_H) + rt2 * 128 + (lane & 15) * 8), tag);
    STAMP(12);
    __builtin_amdgcn_fence(__ATOMIC_ACQUIRE, "agent");
    cfence();
    issAct0();
    wait_vm<16>();
    STAMP(13);
    if (upd_epoch && lane == 0) __hip_atomic_store(flagp(ws, IS_A ? FL_EPOCH_A : FL_EPOCH_B), tag, RLX_AGENT);
  }
  cfence(); bar(); cfence();
#pragma unroll
  for (int s = 1; s < S - 1; ++s) issAct(s, s * SB);
  STAMP(15);
  int wr = (S - 1) * SB;
#pragma unroll 1
  for (int t = 0; t < NT; ++t) {
    if (t < S - 2) { if (t == 0) wait_vm<4 * (S - 2)>(); else if (t == 1) wait_vm<4 * (S - 2) + 4>(); else wait_vm<4 * (S - 2) + 8>(); }
    else wait_vm<8 * (S - 2)>();
    bar(); cfence();
    issAct(t + S - 1, wr); issW(t + S - 1, wr);
    wr += SB; if (wr == S * SB) wr = 0;
  }
  wait_vm<0>();
  STAMP(16);
  STAMP_FLUSH2();
}

DI void publish_half(LAS unsigned* cnt, GAS u64* flag, u64 tag, int lane) {
  if (lane == 0) {
    const unsigned old = __hip_atomic_fetch_add(cnt, 1u, __ATOMIC_RELAXED, __HIP_MEMORY_SCOPE_WORKGROUP);
    if (old == 3u) __hip_atomic_store(flag, tag, RLX_AGENT);
  }
}

template <bool IS_A>
DI void phase2(LAS char* sm, char* ws, const u16* __restrict__ Bx, const u16* __restrict__ Dskip, float* __restrict__ out, int rt2, int ct2,
               int wave, int lane, unsigned (&g_st)[2]) {
  char* g_ws = ws;
  constexpr int NT = IS_A ? 16 : 32;
  constexpr int S = P2S, SB = 32768;
  const int wr_ = wave >> 2, wt = wave & 3;
  const int row0 = rt2 * 128 + (IS_A ? 64 * wr_ : 32 * wt) + (lane >> 3);
  const int col0 = ct2 * 128 + (IS_A ? 32 * wt : 64 * wr_) + 4 * (lane & 7);
  u32x2 xr[2][4], dr[2];
  if constexpr (!IS_A) {
#pragma unroll
    for (int j = 0; j < 2; ++j) {
      dr[j] = *(const u32x2*)(Dskip + col0 + 32 * j);
#pragma unroll
      for (int i = 0; i < 4; ++i) xr[j][i] = *(const u32x2*)(Bx + (size_t)(row0 + 8 * i) * 1024 + col0 + 32 * j);
    }
  }
  cfence(); bar(); cfence();
  STAMP(19);
  const int r16 = lane & 15, g = lane >> 4, q = r16 >> 2, p4 = lane & 3;
  int rofs[2], tofs[2];
#pragma unroll
  for (int s = 0; s < 2; ++s) {
    rofs[s] = (IS_A ? 0 : 16384) + (64 * wr_ + r16) * 128 + 16 * ((4 * s + g) ^ ((r16 >> 1) & 7));
    tofs[s] = (IS_A ? 16384 + (4 * g + q) * 128 : (8 * g + q) * 128) + (wt >> 1) * 8192 + 8 * p4 +
              32 * ((2 * (wt & 1) + s) ^ (((q >> 1) & 1) | ((g & 1) << 1)));
  }
  f32x4 acc[4][2];
#pragma unroll
  for (int i = 0; i < 4; ++i)
#pragma unroll
    for (int j = 0; j < 2; ++j) acc[i][j] = (f32x4){0.f, 0.f, 0.f, 0.f};
  {
    bf16x8 fr[2][4];
    s16x4 tl[2][2][2];
#pragma unroll
    for (int i = 0; i < 4; ++i) { fr[1][i] = zero8(); tl[1][i >> 1][i & 1] = (s16x4){0, 0, 0, 0}; }
    auto mma = [&](auto B_, auto I) {
      constexpr int B = decltype(B_)::v, i = decltype(I)::v, ri = i >> 1, ti = i & 1;
      if constexpr (IS_A) acc[ri][ti] = MFMA16(cat8(tl[B][ti][0], tl[B][ti][1]), fr[B][ri], acc[ri][ti]);
      else acc[ri][ti] = MFMA16(fr[B][ri], cat8(tl[B][ti][0], tl[B][ti][1]), acc[ri][ti]);
    };
    auto rdf = [&](auto B_, auto S_, auto X, LAS char* sb) {
      constexpr int B = decltype(B_)::v, x = decltype(X)::v, s = decltype(S_)::v;
      constexpr int tt = IS_A ? 2048 : 512;
      if constexpr (x == 0) fr[B][0] = lds_b128(sb + rofs[s]);
      else if constexpr (x < 5) tl[B][(x - 1) >> 1][(x - 1) & 1] = lds_tr(sb + tofs[(x - 1) >> 1] + s * 4096 + ((x - 1) & 1) * tt);
      else fr[B][x - 4] = lds_b128(sb + rofs[s] + (x - 4) * 2048);
    };
    int rd = 0;
#pragma unroll 1
    for (int t = 0; t < NT; ++t) {
      wait_lgkm0(); bar(); cfence(); SCHED();
      LAS char* sb = sm + rd;
      mgroup<8, 8, 1>([&](auto i) { mma(IC<1>{}, i); }, [&](auto x) { rdf(IC<0>{}, IC<0>{}, x, sb); });
      mgroup<8, 8, 1>([&](auto i) { mma(IC<0>{}, i); }, [&](auto x) { rdf(IC<1>{}, IC<1>{}, x, sb); });
      rd += SB; if (rd == S * SB) rd = 0;
    }
    sfor<0, 8>([&](auto i) { mma(IC<1>{}, i); });
  }
  STAMP(20);
  {
    const rsrc_t ors = mk_rsrc(out, 2048 * 2048 * 4);
    const int obase = (row0 * 2048 + (IS_A ? 0 : 1024) + col0) * 4;
    LAS char* sc = sm + 131072 + wave * 4096;
    LAS char* wp = sc + r16 * 128;
    LAS char* rp = sc + (lane >> 3) * 128 + (((lane & 7) ^ (lane >> 3)) * 16);
#pragma unroll
    for (int j = 0; j < 2; ++j) {
#pragma unroll
      for (int e = 0; e < 4; ++e) {
        const int ri = 2 * j + (e >> 1), ti = e & 1;
        const int tt_ = IS_A ? (e >> 1) : ti, nt_ = IS_A ? ti : (e >> 1);
        *(LAS f32x4*)(wp + tt_ * 2048 + (((4 * nt_ + g) ^ (lane & 7)) * 16)) = acc[ri][ti];
      }
      wait_lgkm0();
      f32x4 v[4];
#pragma unroll
      for (int i = 0; i < 4; ++i) v[i] = *(LAS f32x4*)(rp + i * 1024);
#pragma unroll
      for (int i = 0; i < 4; ++i) {
        if constexpr (!IS_A) {
          v[i][0] = fmaf(bflo(xr[j][i][0]), bflo(dr[j][0]), v[i][0]); v[i][1] = fmaf(bfhi(xr[j][i][0]), bfhi(dr[j][0]), v[i][1]);
          v[i][2] = fmaf(bflo(xr[j][i][1]), bflo(dr[j][1]), v[i][2]); v[i][3] = fmaf(bfhi(xr[j][i][1]), bfhi(dr[j][1]), v[i][3]);
        }
        __builtin_amdgcn_raw_buffer_store_b128(__builtin_bit_cast(u32x4, v[i]), ors, obase + (IS_A ? j * 32 * 8192 : j * 128) + i * 8 * 8192, 0, OUT_AUX);
      }
      wait_lgkm0();
    }
  }
  STAMP(21);
}

__global__ __launch_bounds__(768, 3) void fused_kernel(
    const u16* __restrict__ Ax, const u16* __restrict__ Wqkv, const u16* __restrict__ Wphi, const u16* __restrict__ Wo,
    const u16* __restrict__ Bx, const u16* __restrict__ Lre, const u16* __restrict__ Lim, const u16* __restrict__ Bre,
    const u16* __restrict__ Bim, const u16* __restrict__ Cre, const u16* __restrict__ Cim, const u16* __restrict__ Logdt,
    const u16* __restrict__ Dskip, float* __restrict__ out, char* ws) {
  __shared__ __attribute__((aligned(1024))) char smem[LDS_BYTES];
  LAS char* sm = (LAS char*)smem;
  char* g_ws = ws;
  unsigned g_st[2] = {0u, 0u};
  STAMP(0);
  const int tid = threadIdx.x, lane = tid & 63, wave = __builtin_amdgcn_readfirstlane(tid >> 6);
  const int b = blockIdx.x, xcd = b & 7, idx = b >> 3;
  const int i16 = idx >> 1;
  const bool isA = (idx & 1) == 0;
  const int rt = 4 * (xcd & 1) + (i16 & 3), cg = 4 * (xcd >> 1) + (i16 >> 2);
  const int rt2 = 4 * (xcd & 3) + (i16 & 3), ct2 = 4 * (xcd >> 2) + (i16 >> 2);
  const int h = lane >> 5, c32 = lane & 31;
  const int rot1 = isA ? 0 : 4 * (xcd >> 1) + 8 * (xcd & 1);
  constexpr int WR_OFF = 98304;
  constexpr int PH_PITCH = 136, PH_BYTES = 64 * PH_PITCH;
  constexpr int RED_OFF = 8 * PH_BYTES;
  constexpr int RED_W = 8192 + 256;
  constexpr int RED_GAP = 6912;
  constexpr int KVF_OFF = 147456;
  constexpr int CNT_OFF = 163824;
  constexpr int SC_OFF = 98304;
  constexpr int SB1 = 49152;
  constexpr int TE_OFF = 3 * SB1;
  constexpr int TP_OFF = 131072;
  constexpr int TC_OFF = TE_OFF + 8192;

  if (wave >= 8) {
    const int lw = wave - 8;
    const int r5 = 8 * lw + (lane >> 3);
    const unsigned rowchunk = (unsigned)(((lane & 7) ^ ((r5 >> 1) & 7)) * 16);
    LAS char* dst0 = sm + lw * 1024;
    if (isA) {
      const unsigned voffA = (unsigned)r5 * 2048 + rowchunk;
      const unsigned voffB = (unsigned)r5 * 6144 + (unsigned)(((lane & 7) ^ ((((r5 >> 1) & 1) | (((r5 >> 3) & 1) << 1)) << 1)) * 16);
      const char* gA = (const char*)Ax + (size_t)rt * 256 * 2048;
      const char* gB = (const char*)Wqkv + (size_t)cg * 128;
      auto issX = [&](int kt, int buf) {
        const rsrc_t r = mk_rsrc(gA, kt < 16 ? 256 * 2048 : 0);
#pragma unroll
        for (int i = 0; i < 8; ++i) dma16(r, dst0 + buf + i * 4096, voffA, ((kt + rot1) & 15) * 128 + i * 32 * 2048);
      };
      auto issW = [&](int j, int kt, int u) {
        const rsrc_t r = mk_rsrc(gB, kt < 16 ? 1024 * 6144 - cg * 128 : 0);
#pragma unroll
        for (int i = 0; i < 2; ++i) dma16(r, dst0 + WR_OFF + (u & 7) * 8192 + i * 4096, voffB, (((kt + rot1) & 15) * 64 + i * 32) * 6144 + j * 2048);
      };
      issX(0, 0); issW(0, 0, 0); issW(1, 0, 1); issW(2, 0, 2);
      u64 epoch = __hip_atomic_load(flagp(ws, FL_EPOCH_A), RLX_AGENT);
      u16 wraw[8];
#pragma unroll
      for (int j = 0; j < 8; ++j) wraw[j] = Wphi[(size_t)(2 * cg + (lw >> 1)) * 1024 + (16 * (j >> 2) + 4 * (lane >> 4) + (j & 3)) * 32 + 16 * (lw & 1) + (lane & 15)];
      cfence();
      issX(1, 32768); issW(0, 1, 3); issW(1, 1, 4);
      int xw = 65536, u0 = 0;
#pragma unroll 1
      for (int t = 0; t < 16; ++t) {
        wait_vm<12>(); bar(); cfence();
        issW(2, t + 1, u0 + 5); issX(t + 2, xw); issW(0, t + 2, u0 + 6); issW(1, t + 2, u0 + 7);
        xw += 32768; if (xw == 98304) xw = 0;
        u0 = (u0 + 3) & 7;
      }
      wait_vm<0>();
      {
        s16x8 w8;
#pragma unroll
        for (int j = 0; j < 8; ++j) w8[j] = (short)wraw[j];
        *(LAS s16x8*)(sm + WR_OFF + 2 * 8192 + lw * 1024 + lane * 16) = w8;
      }
      const u64 tag = epoch + 1ull;
      sync_lds();
      __builtin_amdgcn_s_setprio(3);
      bar(); cfence();
      if (tid == 512) *(LAS u32x4*)(sm + CNT_OFF) = (u32x4){0u, 0u, 0u, 0u};
      STAMP(3);
      {
        const rsrc_t kvrs = mk_rsrc(ws + WS_KVP + (size_t)(cg * 8 + rt) * KVP_BYTES, KVP_BYTES);
#pragma unroll
        for (int j = 0; j < 3; ++j) {
          if (j < 2 || lw == 0) {
            const int zl = lane & 15, zh = zl >> 3;
            f32x4 s0 = {0.f, 0.f, 0.f, 0.f}, s1 = {0.f, 0.f, 0.f, 0.f};
#pragma unroll
            for (int w = 0; w < 4; ++w) {
              if (j < 2) {
                LAS char* rp = sm + RED_OFF + (2 * w + j) * RED_W + (2 * w + j >= 5 ? RED_GAP : 0) + (2 * lw) * 1024 + lane * 16;
                s0 += *(LAS f32x4*)rp; s1 += *(LAS f32x4*)(rp + 1024);
              } else {
                LAS char* rp = sm + RED_OFF + (2 * w + zh) * RED_W + (2 * w + zh >= 5 ? RED_GAP : 0) + 8192 + (zl & 7) * 32;
                s0 += *(LAS f32x4*)rp; s1 += *(LAS f32x4*)(rp + 16);
              }
            }
            const u32x4 d = {pk2(s0[0], s0[1]), pk2(s0[2], s0[3]), pk2(s1[0], s1[1]), pk2(s1[2], s1[3])};
            if (j < 2) __builtin_amdgcn_raw_buffer_store_b128(d, kvrs, ((j * 4 + lw) * 64 + lane) * 16, 0, 16);
            else if (lane < 16) __builtin_amdgcn_raw_buffer_store_b128(d, kvrs, 8192 + zl * 16, 0, 16);
          }
        }
        STAMP(4);
        wait_vm<0>();
        STAMP(5);
        if (lane == 0) __hip_atomic_store(flagp(ws, FL_KV + cg * 256 + rt * 32 + lw * 8), tag, RLX_AGENT);
        if (lw == 0) poll_flag(flagp(ws, FL_KV + cg * 256 + (lane & 31) * 8), tag);
      }
      STAMP(6);
      __builtin_amdgcn_s_setprio(0);
      sync_lds();
      bar(); cfence();
      phase2_load<true>(sm, ws, Wo, Wo, rt2, ct2, lw, lane, 4 * (xcd & 3) + 8 * (xcd >> 2), tag, rt2 == 0 && ct2 == 0, g_st);
    } else {
      const int tloc = 16 * ((r5 >> 2) & 3) + 8 * (r5 & 1) + ((r5 >> 1) & 1) + 2 * (r5 >> 4);
      const unsigned voffA = (unsigned)tloc * 2048 + rowchunk;
      const unsigned voffB = (unsigned)r5 * 2048 + rowchunk;
      const char* gA = (const char*)Bx + (size_t)rt * 256 * 2048;
      const char* gBr = (const char*)Bre + (size_t)cg * 64 * 2048;
      const char* gBi = (const char*)Bim + (size_t)cg * 64 * 2048;
      auto issue = [&](int kt, int buf) {
        const bool live = kt < 16;
        kt = (kt + rot1) & 15;
        const rsrc_t ra = mk_rsrc(gA, live ? 256 * 2048 : 0), rr = mk_rsrc(gBr, live ? 64 * 2048 : 0), ri = mk_rsrc(gBi, live ? 64 * 2048 : 0);
#pragma unroll
        for (int i = 0; i < 8; ++i) dma16(ra, dst0 + buf + i * 4096, voffA, kt * 128 + ((i >> 1) * 64 + (i & 1) * 4) * 2048);
#pragma unroll
        for (int i = 8; i < 12; ++i) dma16(i < 10 ? rr : ri, dst0 + buf + i * 4096, voffB, kt * 128 + (i & 1) * 32 * 2048);
      };
      issue(0, 0);
      u64 epoch = __hip_atomic_load(flagp(ws, FL_EPOCH_B), RLX_AGENT);
      cfence();
      issue(1, SB1);
      int wr = 2 * SB1;
#pragma unroll 1
      for (int t = 0; t < 16; ++t) {
        wait_vm<12>(); bar(); cfence();
        issue(t + 2, wr);
        wr += SB1; if (wr == 3 * SB1) wr = 0;
      }
      wait_vm<0>();
      const u64 tag = epoch + 1ull;
      const unsigned tlo = (unsigned)tag, thi = (unsigned)(tag >> 32);
      STAMP(2);
      bar();
      if (tid == 512) *(LAS u32x4*)(sm + CNT_OFF) = (u32x4){0u, 0u, 0u, 0u};
      STAMP(3);
      if (lw == 0) {
        __builtin_amdgcn_s_setprio(3);
        const f32x4 kc = *(LAS f32x4*)(sm + TC_OFF + lane * 48 + 32);
        f32x2 E[16];
#pragma unroll
        for (int s = 0; s < 16; ++s) E[s] = *(LAS f32x2*)(sm + TE_OFF + (s * 64 + lane) * 8);
        float sr = 0.f, si = 0.f;
#pragma unroll
        for (int s = 0; s < 16; ++s) {
          const float nr = fmaf(kc[0], sr, fmaf(-kc[1], si, E[s][0])), ni = fmaf(kc[0], si, fmaf(kc[1], sr, E[s][1]));
          E[s] = (f32x2){sr, si}; sr = nr; si = ni;
        }
        const u32x4 mine = {__float_as_uint(sr), tlo, __float_as_uint(si), thi};
        __builtin_amdgcn_raw_buffer_store_b128(mine, mk_rsrc(ws + WS_AGG + (size_t)cg * 8192, 8192), (rt * 64 + lane) * 16, 0, 16);
#pragma unroll
        for (int s = 0; s < 16; ++s) *(LAS f32x2*)(sm + TE_OFF + (s * 64 + lane) * 8) = E[s];
        const rsrc_t agrs = mk_rsrc(ws + WS_AGG + (size_t)cg * 8192, rt * 1024);
        u32x4 gr[7];
#pragma unroll 1
        for (unsigned spins = 0; spins < SPIN_CAP; ++spins) {
          unsigned bad = 0;
#pragma unroll
          for (int k = 0; k < 7; ++k) gr[k] = __builtin_amdgcn_raw_buffer_load_b128(agrs, (k * 64 + lane) * 16, 0, 16);
#pragma unroll
          for (int k = 0; k < 7; ++k) if (k < rt) bad |= (gr[k][1] ^ tlo) | (gr[k][3] ^ thi);
          if (__all(bad == 0)) break;
          __builtin_amdgcn_s_sleep(POLL_SLEEP);
        }
        sr = 0.f; si = 0.f;
#pragma unroll
        for (int k = 0; k < 7; ++k)
          if (k < rt) {
            const float nr = fmaf(kc[2], sr, fmaf(-kc[3], si, __uint_as_float(gr[k][0]))), ni = fmaf(kc[2], si, fmaf(kc[3], sr, __uint_as_float(gr[k][2])));
            sr = nr; si = ni;
          }
        *(LAS f32x2*)(sm + TP_OFF + 8192 + lane * 8) = (f32x2){sr, si};
        __builtin_amdgcn_s_setprio(0);
      } else if (lw == 1) {
        const f32x4 kc = *(LAS f32x4*)(sm + TC_OFF + lane * 48 + 32);
        float pr = 1.f, pi = 0.f;
#pragma unroll
        for (int s = 0; s < 16; ++s) {
          *(LAS f32x2*)(sm + TP_OFF + (s * 64 + lane) * 8) = (f32x2){pr, pi};
          const float nr = pr * kc[0] - pi * kc[1], ni = pr * kc[1] + pi * kc[0]; pr = nr; pi = ni;
        }
      }
      STAMP(4);
      sync_lds();
      phase2_load<false>(sm, ws, Cre, Cim, rt2, ct2, lw, lane, 8 * (xcd & 3) + 16 * (xcd >> 2), tag, rt2 == 15 && ct2 == 0, g_st);
    }
    return;
  }

  const int wm = wave >> 1, wn = wave & 1;
  const int xs = (lane >> 1) & 7;
  const int q = (lane & 15) >> 2, p4 = lane & 3, blk = (lane >> 4) & 1;
  int rofs[4];
#pragma unroll
  for (int s = 0; s < 4; ++s) rofs[s] = c32 * 128 + 16 * ((2 * s + h) ^ xs);

  if (isA) {
    u64 epoch = __hip_atomic_load(flagp(ws, FL_EPOCH_A), RLX_AGENT);
    const int r16 = lane & 15, g4 = lane >> 4;
    int ro16[2], tofs[2];
#pragma unroll
    for (int s = 0; s < 2; ++s) {
      ro16[s] = wm * 8192 + r16 * 128 + 16 * ((4 * s + g4) ^ ((r16 >> 1) & 7));
      tofs[s] = WR_OFF + (8 * g4 + q) * 128 + 8 * p4 + 32 * ((2 * wn + s) ^ (((q >> 1) & 1) | ((g4 & 1) << 1)));
    }
    f32x4 Qt[2][4], Kt[2][4], V[4][2];
#pragma unroll
    for (int i = 0; i < 4; ++i)
#pragma unroll
      for (int j = 0; j < 2; ++j) { Qt[j][i] = (f32x4){0.f, 0.f, 0.f, 0.f}; Kt[j][i] = (f32x4){0.f, 0.f, 0.f, 0.f}; V[i][j] = (f32x4){0.f, 0.f, 0.f, 0.f}; }
    STAMP(1);
    {
      bf16x8 fx[4];
      s16x4 fw[6][2];
      fx[2] = zero8(); fx[3] = zero8();
#pragma unroll
      for (int j = 0; j < 6; ++j) { fw[j][0] = (s16x4){0, 0, 0, 0}; fw[j][1] = (s16x4){0, 0, 0, 0}; }
      auto mma = [&](auto TT, auto J) {
        constexpr int tt = decltype(TT)::v, j = decltype(J)::v;
        const bf16x8 w = cat8(fw[j][0], fw[j][1]);
        if constexpr (j < 2) Qt[j][tt] = MFMA16(w, fx[tt], Qt[j][tt]);
        else if constexpr (j < 4) Kt[j - 2][tt] = MFMA16(w, fx[tt], Kt[j - 2][tt]);
        else V[tt][j - 4] = MFMA16(fx[tt], w, V[tt][j - 4]);
      };
      int xr = 0, u0 = 0;
#pragma unroll 1
      for (int t = 0; t < 16; ++t) {
        wait_lgkm0(); bar(); cfence(); SCHED();
        LAS char* xb = sm + xr;
        const int wo_[3] = {(u0 & 7) * 8192, ((u0 + 1) & 7) * 8192, ((u0 + 2) & 7) * 8192};
        auto pass1 = [&](auto NS) {
          constexpr int ns = decltype(NS)::v;
          sfor<0, 6>([&](auto J) {
            constexpr int j = decltype(J)::v;
            mma(IC<2>{}, J); SCHED();
            if constexpr (j < 2) { fx[j] = lds_b128(xb + ro16[ns] + j * 2048); SCHED(); }
            mma(IC<3>{}, J); SCHED();
            LAS char* wp = sm + tofs[j & 1] + wo_[j >> 1] + ns * 4096;
            fw[j][0] = lds_tr(wp); fw[j][1] = lds_tr(wp + 512); SCHED();
          });
        };
        auto pass0 = [&](auto S_) {
          constexpr int s = decltype(S_)::v;
          sfor<0, 6>([&](auto J) {
            constexpr int j = decltype(J)::v;
            mma(IC<0>{}, J); SCHED();
            if constexpr (j < 2) { fx[2 + j] = lds_b128(xb + ro16[s] + (2 + j) * 2048); SCHED(); }
            mma(IC<1>{}, J); SCHED();
          });
        };
        pass1(IC<0>{}); pass0(IC<0>{}); pass1(IC<1>{}); pass0(IC<1>{});
        xr += 32768; if (xr == 98304) xr = 0;
        u0 = (u0 + 3) & 7;
      }
      sfor<0, 6>([&](auto J) { mma(IC<2>{}, J); mma(IC<3>{}, J); });
    }
    asm volatile("" : "+v"(epoch));
    const u64 tag = epoch + 1ull;
    STAMP(2);
    sync_lds();
    STAMP(3);
    const bf16x8 wf0 = lds_b128(sm + WR_OFF + 2 * 8192 + (2 * wn) * 1024 + lane * 16);
    const bf16x8 wf1 = lds_b128(sm + WR_OFF + 2 * 8192 + (2 * wn + 1) * 1024 + lane * 16);
    const f32x4 zero4 = {0.f, 0.f, 0.f, 0.f};
    s16x8 o8;
#pragma unroll
    for (int j = 0; j < 8; ++j) o8[j] = (short)0x3f80;
    const bf16x8 ones = __builtin_bit_cast(bf16x8, o8);
    LAS char* ph = sm + wave * PH_BYTES;
#pragma unroll
    for (int tt = 0; tt < 4; ++tt) {
      const bf16x8 kb = pack44(Kt[0][tt], Kt[1][tt]);
      const f32x4 pk[2] = {MFMA16(wf0, kb, zero4), MFMA16(wf1, kb, zero4)};
      f32x4 pp[2], pm[2];
      dual_exp<true>(pk, pp, pm);
      LAS char* rowp = ph + (16 * tt + r16) * PH_PITCH + 8 * g4;
#pragma unroll
      for (int f = 0; f < 2; ++f) {
        *(LAS u32x2*)(rowp + 32 * f) = (u32x2){pk2(pp[f][0], pp[f][1]), pk2(pp[f][2], pp[f][3])};
        *(LAS u32x2*)(rowp + 64 + 32 * f) = (u32x2){pk2(pm[f][0], pm[f][1]), pk2(pm[f][2], pm[f][3])};
      }
    }
    wait_lgkm0();
    STAMP(4);
    {
      f32x4 KV[4][2], Z[4];
#pragma unroll
      for (int fs = 0; fs < 4; ++fs) { KV[fs][0] = zero4; KV[fs][1] = zero4; Z[fs] = zero4; }
      LAS char* tp = ph + (4 * g4 + q) * PH_PITCH + 8 * p4;
#pragma unroll
      for (int u = 0; u < 2; ++u) {
        const bf16x8 vb0 = pack44(V[2 * u][0], V[2 * u + 1][0]), vb1 = pack44(V[2 * u][1], V[2 * u + 1][1]);
#pragma unroll
        for (int fs = 0; fs < 4; ++fs) {
          LAS char* t0 = tp + 32 * u * PH_PITCH + 32 * fs;
          const bf16x8 af = cat8(lds_tr(t0), lds_tr(t0 + 16 * PH_PITCH));
          KV[fs][0] = MFMA16(af, vb0, KV[fs][0]); KV[fs][1] = MFMA16(af, vb1, KV[fs][1]);
          Z[fs] = MFMA16(af, ones, Z[fs]);
        }
      }
      LAS char* rw = sm + RED_OFF + wave * RED_W + (wave >= 5 ? RED_GAP : 0);
#pragma unroll
      for (int fs = 0; fs < 4; ++fs)
#pragma unroll
        for (int dt = 0; dt < 2; ++dt) *(LAS f32x4*)(rw + ((((fs >> 1) * 2 + dt) * 2) + (fs & 1)) * 1024 + lane * 16) = KV[fs][dt];
      if (r16 == 0) {
#pragma unroll
        for (int fs = 0; fs < 4; ++fs) *(LAS f32x4*)(rw + 8192 + (((fs >> 1) * 4 + g4) * 2 + (fs & 1)) * 16) = Z[fs];
      }
    }
    STAMP(5);
    sync_lds();
    STAMP(6);
    asm volatile("" : "+v"(Qt[0][0]), "+v"(Qt[0][1]), "+v"(Qt[0][2]), "+v"(Qt[0][3]), "+v"(Qt[1][0]), "+v"(Qt[1][1]), "+v"(Qt[1][2]), "+v"(Qt[1][3]));
    bf16x8 fq[4][2];
#pragma unroll
    for (int tt = 0; tt < 4; ++tt) {
      const bf16x8 qb = pack44(Qt[0][tt], Qt[1][tt]);
      const f32x4 pq[2] = {MFMA16(wf0, qb, zero4), MFMA16(wf1, qb, zero4)};
      f32x4 pp[2], pm[2];
      dual_exp<false>(pq, pp, pm);
      fq[tt][0] = pack44(pp[0], pp[1]); fq[tt][1] = pack44(pm[0], pm[1]);
    }
    asm volatile("" : "+v"(fq[0][0]), "+v"(fq[0][1]), "+v"(fq[1][0]), "+v"(fq[1][1]), "+v"(fq[2][0]), "+v"(fq[2][1]), "+v"(fq[3][0]), "+v"(fq[3][1]));
    STAMP(10);
    cfence(); bar(); cfence();
    STAMP(12);
    {
      const rsrc_t kvrs = mk_rsrc(ws + WS_KVP + (size_t)cg * 8 * KVP_BYTES, 8 * KVP_BYTES);
      u32x4 v[8], vz[8];
#pragma unroll
      for (int r = 0; r < 8; ++r) v[r] = __builtin_amdgcn_raw_buffer_load_b128(kvrs, tid * 16, r * KVP_BYTES, 16);
      if (wave == 7) {
#pragma unroll
        for (int r = 0; r < 8; ++r) vz[r] = __builtin_amdgcn_raw_buffer_load_b128(kvrs, 8192 + (lane & 15) * 16, r * KVP_BYTES, 16);
      }
      auto sum8 = [&](const u32x4 (&x)[8]) {
        f32x4 a = {0.f, 0.f, 0.f, 0.f}, c = {0.f, 0.f, 0.f, 0.f};
#pragma unroll
        for (int r = 0; r < 8; ++r) {
          a += (f32x4){bflo(x[r][0]), bfhi(x[r][0]), bflo(x[r][1]), bfhi(x[r][1])};
          c += (f32x4){bflo(x[r][2]), bfhi(x[r][2]), bflo(x[r][3]), bfhi(x[r][3])};
        }
        return (u32x4){pk2(a[0], a[1]), pk2(a[2], a[3]), pk2(c[0], c[1]), pk2(c[2], c[3])};
      };
      *(LAS u32x4*)(sm + KVF_OFF + tid * 16) = sum8(v);
      if (wave == 7) { const u32x4 z = sum8(vz); if (lane < 16) *(LAS u32x4*)(sm + KVF_OFF + 8192 + lane * 16) = z; }
    }
    STAMP(13);
    sync_lds();
    STAMP(14);
    {
      bf16x8 kf[4], zf[2];
#pragma unroll
      for (int ks = 0; ks < 4; ++ks) kf[ks] = lds_b128(sm + KVF_OFF + wn * 4096 + ks * 1024 + lane * 16);
#pragma unroll
      for (int sg = 0; sg < 2; ++sg) {
        const bf16x8 z = lds_b128(sm + KVF_OFF + 8192 + wn * 128 + (sg * 4 + g4) * 16);
        zf[sg] = (lane & 1) ? ones : z;
      }
      LAS char* sc = sm + SC_OFF + wave * 5120;
#pragma unroll
      for (int tt = 0; tt < 4; ++tt) {
        const f32x4 dp = MFMA16(zf[0], fq[tt][0], zero4), dm = MFMA16(zf[1], fq[tt][1], zero4);
        const f32x4 np0 = MFMA16(kf[0], fq[tt][0], zero4), np1 = MFMA16(kf[1], fq[tt][0], zero4);
        const f32x4 nm0 = MFMA16(kf[2], fq[tt][1], zero4), nm1 = MFMA16(kf[3], fq[tt][1], zero4);
        const float rp = __builtin_amdgcn_rcpf(dp[1]), rm = __builtin_amdgcn_rcpf(dm[1]);
        const float rinv = 1.0f / (dp[0] * rp + dm[0] * rm + 1e-6f);
        const float cp = rp * rinv, cm = rm * rinv;
        *(LAS bf16x8*)(sc + (16 * tt + r16) * 80 + g4 * 16) = pack44(np0 * cp + nm0 * cm, np1 * cp + nm1 * cm);
      }
      wait_lgkm0();
      const rsrc_t ors = mk_rsrc(ws + WS_O, 2048 * 2048);
      const int obase = ((2 * cg + wn) * 2048 + rt * 256 + 64 * wm) * 64 + lane * 16;
#pragma unroll
      for (int j = 0; j < 4; ++j) {
        const u32x4 v = *(LAS u32x4*)(sc + (16 * j + (lane >> 2)) * 80 + (lane & 3) * 16);
        __builtin_amdgcn_raw_buffer_store_b128(v, ors, obase + j * 1024, 0, 16);
      }
    }
    STAMP(15);
    wait_vm<0>();
    STAMP(16);
    publish_half((LAS unsigned*)(sm + CNT_OFF) + (wave >> 2), flagp(ws, FL_O + (2 * rt + (wave >> 2)) * 128 + cg * 8), tag, lane);
    phase2<true>(sm, ws, Bx, Dskip, out, rt2, ct2, wave, lane, g_st);
  } else {
    u64 epoch = __hip_atomic_load(flagp(ws, FL_EPOCH_B), RLX_AGENT);
    const uintptr_t pw0 = (uintptr_t)Wqkv, pw1 = (uintptr_t)Cre, pw2 = (uintptr_t)Cim, pw3 = (uintptr_t)Wo;
    if (wave == 7) {
      const int ch = 64 * cg + lane;
      const float lamr = bf2f(Lre[ch]), lami = bf2f(Lim[ch]);
      const float dt = expf(bf2f(Logdt[ch]));
      const float zr = dt * lamr, zi = dt * lami;
      float sn, cs;
      sincosf(zi, &sn, &cs);
      const float mag = expf(zr);
      const float lr = mag * cs, li = mag * sn;
      const float nr = lr - 1.0f, ni = li, dd = 2.0f / (lamr * lamr + lami * lami);
      const float c2r = (nr * lamr + ni * lami) * dd, c2i = (ni * lamr - nr * lami) * dd;
      float pr = lr, pi = li;
#pragma unroll
      for (int i = 0; i < 3; ++i) { const float t = pr * pr - pi * pi; pi = 2.0f * pr * pi; pr = t; }
      const float l8r = pr, l8i = pi;
      { const float t = pr * pr - pi * pi; pi = 2.0f * pr * pi; pr = t; }
      const float l16r = pr, l16i = pi;
#pragma unroll
      for (int i = 0; i < 4; ++i) { const float t = pr * pr - pi * pi; pi = 2.0f * pr * pi; pr = t; }
      LAS f32x4* tc = (LAS f32x4*)(sm + TC_OFF + lane * 48);
      tc[0] = (f32x4){lr, li, c2r, c2i}; tc[1] = (f32x4){l8r, l8i, 0.f, 0.f}; tc[2] = (f32x4){l16r, l16i, pr, pi};
    }
    const int r16 = lane & 15, g4 = lane >> 4;
    int ro16[2];
#pragma unroll
    for (int s = 0; s < 2; ++s) ro16[s] = r16 * 128 + 16 * ((4 * s + g4) ^ ((r16 >> 1) & 7));
    f32x4 U[4][4];
#pragma unroll
    for (int i = 0; i < 4; ++i)
#pragma unroll
      for (int j = 0; j < 4; ++j) U[i][j] = (f32x4){0.f, 0.f, 0.f, 0.f};
    STAMP(1);
    {
      bf16x8 fx[2][4], fb[2][4];
#pragma unroll
      for (int i = 0; i < 4; ++i) { fx[1][i] = zero8(); fb[1][i] = zero8(); }
      auto mma = [&](auto B_, auto I) {
        constexpr int B = decltype(B_)::v, i = decltype(I)::v;
        U[i >> 2][i & 3] = MFMA16(fx[B][i >> 2], fb[B][i & 3], U[i >> 2][i & 3]);
      };
      int rd = 0;
#pragma unroll 1
      for (int t = 0; t < 16; ++t) {
        wait_lgkm0(); bar(); cfence(); SCHED();
        if ((t & 7) == wave && t < 12) {
          const uintptr_t wb_ = t < 6 ? pw0 : (t < 8 ? pw1 : (t < 10 ? pw2 : pw3));
          l2touch(mk_rsrc((const void*)wb_, 0x7fffffff), (unsigned)lane * 128, ((t < 6 ? t : (t & 1)) * 128 + xcd * 16 + i16) * 8192);
        }
        LAS char* xb = sm + rd + wm * 8192;
        LAS char* bb = sm + rd + 32768 + wn * 4096;
        auto rdf = [&](auto B_, auto S_, auto X) {
          constexpr int B = decltype(B_)::v, s = decltype(S_)::v, x = decltype(X)::v;
          if constexpr (x == 0) fx[B][0] = lds_b128(xb + ro16[s]);
          else if constexpr (x < 5) fb[B][x - 1] = lds_b128(bb + ro16[s] + ((x - 1) >> 1) * 8192 + ((x - 1) & 1) * 2048);
          else fx[B][x - 4] = lds_b128(xb + ro16[s] + (x - 4) * 2048);
        };
        mgroup<16, 8, 1>([&](auto i) { mma(IC<1>{}, i); }, [&](auto x) { rdf(IC<0>{}, IC<0>{}, x); });
        mgroup<16, 8, 1>([&](auto i) { mma(IC<0>{}, i); }, [&](auto x) { rdf(IC<1>{}, IC<1>{}, x); });
        rd += SB1; if (rd == 3 * SB1) rd = 0;
      }
      sfor<0, 16>([&](auto i) { mma(IC<1>{}, i); });
    }
    asm volatile("" : "+v"(epoch));
    const u64 tag = epoch + 1ull;
    STAMP(2);
    const int seg = 4 * wm + g4, chl = 32 * wn + r16;
    f32x4 k0[2]; f32x2 k8[2];
#pragma unroll
    for (int n = 0; n < 2; ++n) { k0[n] = *(LAS f32x4*)(sm + TC_OFF + (chl + 16 * n) * 48); k8[n] = *(LAS f32x2*)(sm + TC_OFF + (chl + 16 * n) * 48 + 16); }
    f32x2 e0[2];
#pragma unroll
    for (int n = 0; n < 2; ++n) {
      const f32x2 lr2 = {k0[n][0], k0[n][0]}, li2 = {k0[n][1], k0[n][1]};
      f32x2 er = {0.f, 0.f}, ei = {0.f, 0.f};
#pragma unroll
      for (int sg = 0; sg < 8; ++sg) {
        const f32x2 ur = {U[sg >> 1][n][2 * (sg & 1)], U[sg >> 1][n][2 * (sg & 1) + 1]}, ui = {U[sg >> 1][2 + n][2 * (sg & 1)], U[sg >> 1][2 + n][2 * (sg & 1) + 1]};
        const f32x2 nr = __builtin_elementwise_fma(lr2, er, __builtin_elementwise_fma(-li2, ei, ur));
        const f32x2 ni = __builtin_elementwise_fma(lr2, ei, __builtin_elementwise_fma(li2, er, ui));
        er = nr; ei = ni;
        U[sg >> 1][n][2 * (sg & 1)] = er[0]; U[sg >> 1][n][2 * (sg & 1) + 1] = er[1];
        U[sg >> 1][2 + n][2 * (sg & 1)] = ei[0]; U[sg >> 1][2 + n][2 * (sg & 1) + 1] = ei[1];
      }
      e0[n] = (f32x2){er[0], ei[0]};
      *(LAS f32x2*)(sm + TE_OFF + (seg * 64 + chl + 16 * n) * 8) =
          (f32x2){fmaf(k8[n][0], er[0], fmaf(-k8[n][1], ei[0], er[1])), fmaf(k8[n][0], ei[0], fmaf(k8[n][1], er[0], ei[1]))};
    }
    STAMP(3);
    sync_lds();
    STAMP(4);
#pragma unroll
    for (int i = 0; i < 4; ++i) asm volatile("" : "+v"(U[i][0]), "+v"(U[i][1]), "+v"(U[i][2]), "+v"(U[i][3]));
    asm volatile("" : "+v"(k0[0]), "+v"(k0[1]));
    float Pr[2][8], Pi[2][8];
#pragma unroll
    for (int n = 0; n < 2; ++n) {
      const f32x2 cr2 = {k0[n][2], k0[n][2]}, ci2 = {k0[n][3], k0[n][3]};
      float pr = k0[n][2], pi = k0[n][3];
#pragma unroll
      for (int sg = 0; sg < 8; ++sg) {
        const f32x2 er = {U[sg >> 1][n][2 * (sg & 1)], U[sg >> 1][n][2 * (sg & 1) + 1]}, ei = {U[sg >> 1][2 + n][2 * (sg & 1)], U[sg >> 1][2 + n][2 * (sg & 1) + 1]};
        const f32x2 hr = __builtin_elementwise_fma(cr2, er, -(ci2 * ei)), hi = -__builtin_elementwise_fma(cr2, ei, ci2 * er);
        U[sg >> 1][n][2 * (sg & 1)] = hr[0]; U[sg >> 1][n][2 * (sg & 1) + 1] = hr[1];
        U[sg >> 1][2 + n][2 * (sg & 1)] = hi[0]; U[sg >> 1][2 + n][2 * (sg & 1) + 1] = hi[1];
        const float nr = pr * k0[n][0] - pi * k0[n][1], ni = pr * k0[n][1] + pi * k0[n][0];
        pr = nr; pi = ni; Pr[n][sg] = pr; Pi[n][sg] = pi;
      }
    }
#pragma unroll
    for (int i = 0; i < 4; ++i) asm volatile("" : "+v"(U[i][0]), "+v"(U[i][1]), "+v"(U[i][2]), "+v"(U[i][3]));
    asm volatile("" : "+v"(Pr[0][7]), "+v"(Pr[1][7]), "+v"(Pi[0][7]), "+v"(Pi[1][7]));
    sync_lds();
    STAMP(6);
    {
      const rsrc_t hrs = mk_rsrc(ws + WS_H, 2048 * 4096);
      LAS char* sre = sm + SC_OFF + wave * 4096;
      LAS char* sim = sm + 32768 + (wave >> 2) * 32768 + (wave & 3) * 4096;
#pragma unroll
      for (int n = 0; n < 2; ++n) {
        const f32x2 xl = *(LAS f32x2*)(sm + TE_OFF + (seg * 64 + chl + 16 * n) * 8), pw = *(LAS f32x2*)(sm + TP_OFF + (seg * 64 + chl + 16 * n) * 8);
        const f32x2 hin = *(LAS f32x2*)(sm + TP_OFF + 8192 + (chl + 16 * n) * 8);
        const f32x2 s0 = {fmaf(pw[0], hin[0], fmaf(-pw[1], hin[1], xl[0])), fmaf(pw[0], hin[1], fmaf(pw[1], hin[0], xl[1]))};
        const f32x2 sr = {s0[0], fmaf(k8[n][0], s0[0], fmaf(-k8[n][1], s0[1], e0[n][0]))};
        const f32x2 si = {s0[1], fmaf(k8[n][0], s0[1], fmaf(k8[n][1], s0[0], e0[n][1]))};
        f32x2 hr[8], hi[8];
#pragma unroll
        for (int sg = 0; sg < 8; ++sg) {
          const f32x2 ar = {U[sg >> 1][n][2 * (sg & 1)], U[sg >> 1][n][2 * (sg & 1) + 1]}, ai = {U[sg >> 1][2 + n][2 * (sg & 1)], U[sg >> 1][2 + n][2 * (sg & 1) + 1]};
          const f32x2 pr2 = {Pr[n][sg], Pr[n][sg]}, pi2 = {Pi[n][sg], Pi[n][sg]};
          hr[sg] = __builtin_elementwise_fma(pr2, sr, __builtin_elementwise_fma(-pi2, si, ar));
          hi[sg] = __builtin_elementwise_fma(-pr2, si, __builtin_elementwise_fma(-pi2, sr, ai));
        }
#pragma unroll
        for (int c = 0; c < 2; ++c) {
          const int off = (16 * n + r16) * 128 + (((2 * g4 + c) ^ (lane & 7)) * 16);
          *(LAS u32x4*)(sre + off) = (u32x4){pk2(hr[0][c], hr[1][c]), pk2(hr[2][c], hr[3][c]), pk2(hr[4][c], hr[5][c]), pk2(hr[6][c], hr[7][c])};
          *(LAS u32x4*)(sim + off) = (u32x4){pk2(hi[0][c], hi[1][c]), pk2(hi[2][c], hi[3][c]), pk2(hi[4][c], hi[5][c]), pk2(hi[6][c], hi[7][c])};
        }
      }
      STAMP(5);
      wait_lgkm0();
      const int ro = (lane >> 3) * 128 + (((lane & 7) ^ (lane >> 3)) * 16);
      const int hbase = (cg * 64 + 32 * wn + (lane >> 3)) * 4096 + rt * 512 + wm * 128 + (lane & 7) * 16;
      u32x4 v[8];
#pragma unroll
      for (int j = 0; j < 4; ++j) { v[j] = *(LAS u32x4*)(sre + ro + j * 1024); v[4 + j] = *(LAS u32x4*)(sim + ro + j * 1024); }
#pragma unroll
      for (int j = 0; j < 8; ++j) __builtin_amdgcn_raw_buffer_store_b128(v[j], hrs, hbase + ((j >> 2) * 1024 + 8 * (j & 3)) * 4096, 0, 16);
    }
    STAMP(15);
    wait_vm<0>();
    STAMP(16);
    publish_half((LAS unsigned*)(sm + CNT_OFF) + (wave >> 2), flagp(ws, FL_H + (2 * rt + (wave >> 2)) * 128 + cg * 8), tag, lane);
    phase2<false>(sm, ws, Bx, Dskip, out, rt2, ct2, wave, lane, g_st);
  }
}

extern "C" void kernel_launch(void* const* d_in, const int* in_sizes, int n_in, void* d_out, int out_size, void* d_ws,
                              size_t ws_size, hipStream_t stream) {
  fused_kernel<<<256, 768, 0, stream>>>((const u16*)d_in[0], (const u16*)d_in[1], (const u16*)d_in[2], (const u16*)d_in[3],
                                        (const u16*)d_in[4], (const u16*)d_in[5], (const u16*)d_in[6], (const u16*)d_in[7],
                                        (const u16*)d_in[8], (const u16*)d_in[9], (const u16*)d_in[10], (const u16*)d_in[11],
                                        (const u16*)d_in[12], (float*)d_out, (char*)d_ws);
}
```

```cpp
#include <hip/hip_runtime.h>
#include <stdint.h>

#define LAS __attribute__((address_space(3)))
#define GAS __attribute__((address_space(1)))
#define DI __device__ __forceinline__

typedef unsigned short u16;
typedef unsigned long long u64;
typedef __bf16 bf16x8 __attribute__((ext_vector_type(8)));
typedef __bf16 bf16x2 __attribute__((ext_vector_type(2)));
typedef short s16x4 __attribute__((ext_vector_type(4)));
typedef short s16x8 __attribute__((ext_vector_type(8)));
typedef float f32x2 __attribute__((ext_vector_type(2)));
typedef float f32x4 __attribute__((ext_vector_type(4)));
typedef unsigned u32x2 __attribute__((ext_vector_type(2)));
typedef unsigned u32x4 __attribute__((ext_vector_type(4)));

#define MFMA16(a, b, c) __builtin_amdgcn_mfma_f32_16x16x32_bf16((a), (b), (c), 0, 0, 0)
#define RLX_AGENT __ATOMIC_RELAXED, __HIP_MEMORY_SCOPE_AGENT

constexpr size_t WS_O    = 0;
constexpr size_t WS_H    = 4u << 20;
constexpr size_t WS_KVP  = 12u << 20;
constexpr int    KVP_BYTES = 8448;
constexpr size_t WS_AGG  = 15u << 20;
constexpr size_t WS_FLAG = 16u << 20;
constexpr int FL_EPOCH_A = 0, FL_EPOCH_B = 256;
constexpr int FL_KV = 4096;
constexpr int FL_O = 32768;
constexpr int FL_H = 65536;
constexpr int LDS_BYTES = 160 * 1024;
constexpr unsigned SPIN_CAP = 1u << 20;
constexpr int POLL_SLEEP = 2;
constexpr int OUT_AUX = 2;
constexpr int P2S = 4;

#define STAMP_FLUSH2() do {} while (0)
#define STAMP(i) do {} while (0)
#define STAMP_FLUSH() do {} while (0)

DI float bf2f(u16 v) { return __uint_as_float((unsigned)v << 16); }
DI float bflo(unsigned v) { return __uint_as_float(v << 16); }
DI float bfhi(unsigned v) { return __uint_as_float(v & 0xffff0000u); }
DI unsigned pk2(float lo, float hi) { f32x2 x = {lo, hi}; return __builtin_bit_cast(unsigned, __builtin_convertvector(x, bf16x2)); }
template <int N> DI void wait_vm() { asm volatile("s_waitcnt vmcnt(%0)" ::"i"(N) : "memory"); }
DI void wait_lgkm0() { asm volatile("s_waitcnt lgkmcnt(0)" ::: "memory"); }
DI void bar() { __builtin_amdgcn_s_barrier(); }
DI void cfence() { asm volatile("" ::: "memory"); }
DI void sync_lds() { wait_lgkm0(); bar(); cfence(); }
DI bf16x8 lds_b128(LAS char* p) { return *(LAS bf16x8*)p; }
DI s16x4 lds_tr(LAS char* p) { return __builtin_amdgcn_ds_read_tr16_b64_v4i16((LAS s16x4*)p); }
DI bf16x8 cat8(s16x4 lo, s16x4 hi) { return __builtin_bit_cast(bf16x8, __builtin_shufflevector(lo, hi, 0, 1, 2, 3, 4, 5, 6, 7)); }
DI bf16x8 zero8() { s16x8 z = {0, 0, 0, 0, 0, 0, 0, 0}; return __builtin_bit_cast(bf16x8, z); }
DI GAS u64* flagp(char* ws, int off) { return (GAS u64*)(ws + WS_FLAG + off); }
DI float vmax(float a, float b) { return __builtin_elementwise_maximum(a, b); }
DI float vmin(float a, float b) { return __builtin_elementwise_minimum(a, b); }

#define SCHED() __builtin_amdgcn_sched_barrier(0)
template <int I> struct IC { static constexpr int v = I; };
template <int B, int E, class F> DI void sfor(F f) { if constexpr (B < E) { f(IC<B>{}); sfor<B + 1, E>(f); } }
typedef __amdgpu_buffer_rsrc_t rsrc_t;
DI rsrc_t mk_rsrc(const void* p, int bytes) { return __builtin_amdgcn_make_buffer_rsrc((void*)p, 0, bytes, 0x00020000); }
DI void dma16(rsrc_t r, LAS char* l, unsigned voff, int soff) {
  asm volatile("s_mov_b32 m0, %0\n\ts_nop 0\n\tbuffer_load_dwordx4 %1, %2, %3 offen lds"
               ::"s"((unsigned)(size_t)l), "v"(voff), "s"(r), "s"(soff) : "memory");
}
DI void l2touch(rsrc_t r, unsigned voff, int soff) {
  asm volatile("buffer_load_dword v167, %0, %1, %2 offen" ::"v"(voff), "s"(r), "s"(soff) : "memory", "v167");
}
template <int NM, int NR, int RPM, class M, class R>
DI void mgroup(M mma, R rd) {
  sfor<0, NM>([&](auto i) {
    constexpr int I = decltype(i)::v;
    mma(i); SCHED();
    sfor<0, RPM>([&](auto j) { constexpr int X = I * RPM + decltype(j)::v; if constexpr (X < NR) rd(IC<X>{}); });
    SCHED();
  });
}

DI bf16x8 pack44(const f32x4& a, const f32x4& b) {
  return __builtin_bit_cast(bf16x8, (u32x4){pk2(a[0], a[1]), pk2(a[2], a[3]), pk2(b[0], b[1]), pk2(b[2], b[3])});
}
template <bool MAX> DI void red2(float& a, float& b) {
  auto r = __builtin_amdgcn_permlane32_swap(__float_as_uint(a), __float_as_uint(b), false, false);
  float c = MAX ? vmax(__uint_as_float(r[0]), __uint_as_float(r[1])) : __uint_as_float(r[0]) + __uint_as_float(r[1]);
  auto r2 = __builtin_amdgcn_permlane16_swap(__float_as_uint(c), __float_as_uint(c), false, false);
  c = MAX ? vmax(__uint_as_float(r2[0]), __uint_as_float(r2[1])) : __uint_as_float(r2[0]) + __uint_as_float(r2[1]);
  auto r3 = __builtin_amdgcn_permlane32_swap(__float_as_uint(c), __float_as_uint(c), false, false);
  a = __uint_as_float(r3[0]); b = __uint_as_float(r3[1]);
}
template <bool NORM> DI void dual_exp(const f32x4 (&x)[2], f32x4 (&pp)[2], f32x4 (&pm)[2]) {
  float mx = vmax(vmax(vmax(x[0][0], x[0][1]), x[0][2]), vmax(vmax(x[0][3], x[1][0]), x[1][1]));
  float mn = vmin(vmin(vmin(x[0][0], x[0][1]), x[0][2]), vmin(vmin(x[0][3], x[1][0]), x[1][1]));
  mx = vmax(vmax(mx, x[1][2]), x[1][3]); mn = -vmin(vmin(mn, x[1][2]), x[1][3]);
  red2<true>(mx, mn);
  const float L2E = 1.4426950408889634f;
  const float nmx = -mx * L2E, nmn = -mn * L2E;
  float sp = 0.f, sm = 0.f;
#pragma unroll
  for (int f = 0; f < 2; ++f)
#pragma unroll
    for (int i = 0; i < 4; ++i) {
      pp[f][i] = __builtin_amdgcn_exp2f(fmaf(x[f][i], L2E, nmx));
      pm[f][i] = __builtin_amdgcn_exp2f(fmaf(x[f][i], -L2E, nmn));
      if constexpr (NORM) { sp += pp[f][i]; sm += pm[f][i]; }
    }
  if constexpr (NORM) {
    red2<false>(sp, sm);
    const float rp = __builtin_amdgcn_rcpf(sp), rm = __builtin_amdgcn_rcpf(sm);
#pragma unroll
    for (int f = 0; f < 2; ++f) { pp[f] *= rp; pm[f] *= rm; }
  }
}

DI void poll_flag(GAS u64* p, u64 tag) {
#pragma unroll 1
  for (unsigned spins = 0; spins < SPIN_CAP; ++spins) {
    const u64 v = __hip_atomic_load(p, RLX_AGENT);
    if (__all(v == tag)) break;
    __builtin_amdgcn_s_sleep(POLL_SLEEP);
  }
}

template <bool IS_A>
DI void phase2_load(LAS char* sm, char* ws, const u16* __restrict__ W0, const u16* __restrict__ W1, int rt2, int ct2, int lw, int lane,
                    int rot, u64 tag, bool upd_epoch, unsigned (&g_st)[2]) {
  constexpr int NT = IS_A ? 16 : 32;
  constexpr int S = P2S, SB = 32768;
  const int r5 = 8 * lw + (lane >> 3);
  const unsigned voffR = (unsigned)r5 * 2048 + (unsigned)(((lane & 7) ^ ((r5 >> 1) & 7)) * 16);
  auto voffTw = [&](int w) {
    const int k = 8 * w + (lane >> 3), f2 = ((k >> 1) & 1) | (((k >> (IS_A ? 2 : 3)) & 1) << 1);
    return (unsigned)k * (IS_A ? 2048 : 4096) + (unsigned)(((lane & 7) ^ (f2 << 1)) * 16);
  };
  const unsigned voffT = voffTw(lw);
  const char* gAct = IS_A ? ws + WS_O + (size_t)rt2 * 128 * 64 : ws + WS_H + (size_t)rt2 * 256;
  const int actBytes = IS_A ? 2048 * 2048 - rt2 * 128 * 64 : 2048 * 4096 - rt2 * 256;
  const int oc = (lane & 7) ^ ((r5 >> 1) & 7);
  const unsigned voffO = (unsigned)(oc >> 2) * (2048 * 64) + (unsigned)r5 * 64 + (unsigned)(oc & 3) * 16;
  const char* gW0 = IS_A ? (const char*)W0 + (size_t)ct2 * 256 : (const char*)W0 + (size_t)ct2 * 128 * 2048;
  const char* gW1 = IS_A ? gW0 : (const char*)W1 + (size_t)ct2 * 128 * 2048;
  const int wBytes = IS_A ? 1024 * 2048 - ct2 * 256 : 128 * 2048;
  LAS char* dst0 = sm + lw * 1024;
  auto issAct = [&](int kt, int buf) {
    const rsrc_t r = mk_rsrc(gAct, kt < NT ? actBytes : 0);
    kt = (kt + rot) & (NT - 1);
#pragma unroll
    for (int i = 0; i < 4; ++i) {
      if constexpr (IS_A) dma16(r, dst0 + buf + i * 4096, voffO, (kt * 2 * 2048 + i * 32) * 64);
      else dma16(r, dst0 + buf + i * 4096, voffT, (kt * 64 + (i & 1) * 32) * 4096 + (i >> 1) * 128);
    }
  };
  auto issAct0 = [&]() {
    const rsrc_t r = mk_rsrc(gAct, actBytes);
    const int kt = rot & (NT - 1);
#pragma unroll
    for (int w = 0; w < 4; ++w) {
      const int ocw = (lane & 7) ^ ((4 * w + (lane >> 4)) & 7);
      const unsigned vo = (unsigned)(ocw >> 2) * (2048 * 64) + (unsigned)(8 * w + (lane >> 3)) * 64 + (unsigned)(ocw & 3) * 16;
#pragma unroll
      for (int i = 0; i < 4; ++i) {
        if constexpr (IS_A) dma16(r, sm + w * 1024 + i * 4096, vo, (kt * 2 * 2048 + i * 32) * 64);
        else dma16(r, sm + w * 1024 + i * 4096, voffTw(w), (kt * 64 + (i & 1) * 32) * 4096 + (i >> 1) * 128);
      }
    }
  };
  auto issW = [&](int kt, int buf) {
    const bool live = kt < NT;
    kt = (kt + rot) & (NT - 1);
    const rsrc_t r = mk_rsrc((IS_A || kt < 16) ? gW0 : gW1, live ? wBytes : 0);
#pragma unroll
    for (int i = 0; i < 4; ++i) {
      if constexpr (IS_A) dma16(r, dst0 + buf + 16384 + i * 4096, voffT, (kt * 64 + (i & 1) * 32) * 2048 + (i >> 1) * 128);
      else dma16(r, dst0 + buf + 16384 + i * 4096, voffR, (kt & 15) * 128 + i * 32 * 2048);
    }
  };
  STAMP(10);
#pragma unroll
  for (int s = 0; s < S - 1; ++s) issW(s, s * SB);
  STAMP(11);
  if (lw == 0) {
    poll_flag(flagp(ws, (IS_A ? FL_O : FL_H) + rt2 * 128 + (lane & 15) * 8), tag);
    STAMP(12);
    __builtin_amdgcn_fence(__ATOMIC_ACQUIRE, "agent");
    cfence();
    issAct0();
    wait_vm<16>();
    STAMP(13);
    if (upd_epoch && lane == 0) __hip_atomic_store(flagp(ws, IS_A ? FL_EPOCH_A : FL_EPOCH_B), tag, RLX_AGENT);
  }
  cfence(); bar(); cfence();
#pragma unroll
  for (int s = 1; s < S - 1; ++s) issAct(s, s * SB);
  STAMP(15);
  int wr = (S - 1) * SB;
#pragma unroll 1
  for (int t = 0; t < NT; ++t) {
    if (t < S - 2) { if (t == 0) wait_vm<4 * (S - 2)>(); else if (t == 1) wait_vm<4 * (S - 2) + 4>(); else wait_vm<4 * (S - 2) + 8>(); }
    else wait_vm<8 * (S - 2)>();
    bar(); cfence();
    issAct(t + S - 1, wr); issW(t + S - 1, wr);
    wr += SB; if (wr == S * SB) wr = 0;
  }
  wait_vm<0>();
  STAMP(16);
  STAMP_FLUSH2();
}

DI void publish_half(LAS unsigned* cnt, GAS u64* flag, u64 tag, int lane) {
  if (lane == 0) {
    const unsigned old = __hip_atomic_fetch_add(cnt, 1u, __ATOMIC_RELAXED, __HIP_MEMORY_SCOPE_WORKGROUP);
    if (old == 3u) __hip_atomic_store(flag, tag, RLX_AGENT);
  }
}

template <bool IS_A>
DI void phase2(LAS char* sm, char* ws, const u16* __restrict__ Bx, const u16* __restrict__ Dskip, float* __restrict__ out, int rt2, int ct2,
               int wave, int lane, unsigned (&g_st)[2]) {
  char* g_ws = ws;
  constexpr int NT = IS_A ? 16 : 32;
  constexpr int S = P2S, SB = 32768;
  const int wr_ = wave >> 2, wt = wave & 3;
  const int row0 = rt2 * 128 + (IS_A ? 64 * wr_ : 32 * wt) + (lane >> 3);
  const int col0 = ct2 * 128 + (IS_A ? 32 * wt : 64 * wr_) + 4 * (lane & 7);
  u32x2 xr[2][4], dr[2];
  if constexpr (!IS_A) {
#pragma unroll
    for (int j = 0; j < 2; ++j) {
      dr[j] = *(const u32x2*)(Dskip + col0 + 32 * j);
#pragma unroll
      for (int i = 0; i < 4; ++i) xr[j][i] = *(const u32x2*)(Bx + (size_t)(row0 + 8 * i) * 1024 + col0 + 32 * j);
    }
  }
  cfence(); bar(); cfence();
  STAMP(19);
  const int r16 = lane & 15, g = lane >> 4, q = r16 >> 2, p4 = lane & 3;
  int rofs[2], tofs[2];
#pragma unroll
  for (int s = 0; s < 2; ++s) {
    rofs[s] = (IS_A ? 0 : 16384) + (64 * wr_ + r16) * 128 + 16 * ((4 * s + g) ^ ((r16 >> 1) & 7));
    tofs[s] = (IS_A ? 16384 + (4 * g + q) * 128 : (8 * g + q) * 128) + (wt >> 1) * 8192 + 8 * p4 +
              32 * ((2 * (wt & 1) + s) ^ (((q >> 1) & 1) | ((g & 1) << 1)));
  }
  f32x4 acc[4][2];
#pragma unroll
  for (int i = 0; i < 4; ++i)
#pragma unroll
    for (int j = 0; j < 2; ++j) acc[i][j] = (f32x4){0.f, 0.f, 0.f, 0.f};
  {
    bf16x8 fr[2][4];
    s16x4 tl[2][2][2];
#pragma unroll
    for (int i = 0; i < 4; ++i) { fr[1][i] = zero8(); tl[1][i >> 1][i & 1] = (s16x4){0, 0, 0, 0}; }
    auto mma = [&](auto B_, auto I) {
      constexpr int B = decltype(B_)::v, i = decltype(I)::v, ri = i >> 1, ti = i & 1;
      if constexpr (IS_A) acc[ri][ti] = MFMA16(cat8(tl[B][ti][0], tl[B][ti][1]), fr[B][ri], acc[ri][ti]);
      else acc[ri][ti] = MFMA16(fr[B][ri], cat8(tl[B][ti][0], tl[B][ti][1]), acc[ri][ti]);
    };
    auto rdf = [&](auto B_, auto S_, auto X, LAS char* sb) {
      constexpr int B = decltype(B_)::v, x = decltype(X)::v, s = decltype(S_)::v;
      constexpr int tt = IS_A ? 2048 : 512;
      if constexpr (x == 0) fr[B][0] = lds_b128(sb + rofs[s]);
      else if constexpr (x < 5) tl[B][(x - 1) >> 1][(x - 1) & 1] = lds_tr(sb + tofs[(x - 1) >> 1] + s * 4096 + ((x - 1) & 1) * tt);
      else fr[B][x - 4] = lds_b128(sb + rofs[s] + (x - 4) * 2048);
    };
    int rd = 0;
#pragma unroll 1
    for (int t = 0; t < NT; ++t) {
      wait_lgkm0(); bar(); cfence(); SCHED();
      LAS char* sb = sm + rd;
      mgroup<8, 8, 1>([&](auto i) { mma(IC<1>{}, i); }, [&](auto x) { rdf(IC<0>{}, IC<0>{}, x, sb); });
      mgroup<8, 8, 1>([&](auto i) { mma(IC<0>{}, i); }, [&](auto x) { rdf(IC<1>{}, IC<1>{}, x, sb); });
      rd += SB; if (rd == S * SB) rd = 0;
    }
    sfor<0, 8>([&](auto i) { mma(IC<1>{}, i); });
  }
  STAMP(20);
  {
    const rsrc_t ors = mk_rsrc(out, 2048 * 2048 * 4);
    const int obase = (row0 * 2048 + (IS_A ? 0 : 1024) + col0) * 4;
    LAS char* sc = sm + 131072 + wave * 4096;
    LAS char* wp = sc + r16 * 128;
    LAS char* rp = sc + (lane >> 3) * 128 + (((lane & 7) ^ (lane >> 3)) * 16);
#pragma unroll
    for (int j = 0; j < 2; ++j) {
#pragma unroll
      for (int e = 0; e < 4; ++e) {
        const int ri = 2 * j + (e >> 1), ti = e & 1;
        const int tt_ = IS_A ? (e >> 1) : ti, nt_ = IS_A ? ti : (e >> 1);
        *(LAS f32x4*)(wp + tt_ * 2048 + (((4 * nt_ + g) ^ (lane & 7)) * 16)) = acc[ri][ti];
      }
      wait_lgkm0();
      f32x4 v[4];
#pragma unroll
      for (int i = 0; i < 4; ++i) v[i] = *(LAS f32x4*)(rp + i * 1024);
#pragma unroll
      for (int i = 0; i < 4; ++i) {
        if constexpr (!IS_A) {
          v[i][0] = fmaf(bflo(xr[j][i][0]), bflo(dr[j][0]), v[i][0]); v[i][1] = fmaf(bfhi(xr[j][i][0]), bfhi(dr[j][0]), v[i][1]);
          v[i][2] = fmaf(bflo(xr[j][i][1]), bflo(dr[j][1]), v[i][2]); v[i][3] = fmaf(bfhi(xr[j][i][1]), bfhi(dr[j][1]), v[i][3]);
        }
        __builtin_amdgcn_raw_buffer_store_b128(__builtin_bit_cast(u32x4, v[i]), ors, obase + (IS_A ? j * 32 * 8192 : j * 128) + i * 8 * 8192, 0, OUT_AUX);
      }
      wait_lgkm0();
    }
  }
  STAMP(21);
}

__global__ __launch_bounds__(768, 3) void fused_kernel(
    const u16* __restrict__ Ax, const u16* __restrict__ Wqkv, const u16* __restrict__ Wphi, const u16* __restrict__ Wo,
    const u16* __restrict__ Bx, const u16* __restrict__ Lre, const u16* __restrict__ Lim, const u16* __restrict__ Bre,
    const u16* __restrict__ Bim, const u16* __restrict__ Cre, const u16* __restrict__ Cim, const u16* __restrict__ Logdt,
    const u16* __restrict__ Dskip, float* __restrict__ out, char* ws) {
  __shared__ __attribute__((aligned(1024))) char smem[LDS_BYTES];
  LAS char* sm = (LAS char*)smem;
  char* g_ws = ws;
  unsigned g_st[2] = {0u, 0u};
  STAMP(0);
  const int tid = threadIdx.x, lane = tid & 63, wave = __builtin_amdgcn_readfirstlane(tid >> 6);
  const int b = blockIdx.x, xcd = b & 7, idx = b >> 3;
  const int i16 = idx >> 1;
  const bool isA = (idx & 1) == 0;
  const int rt = 4 * (xcd & 1) + (i16 & 3), cg = 4 * (xcd >> 1) + (i16 >> 2);
  const int rt2 = 4 * (xcd & 3) + (i16 & 3), ct2 = 4 * (xcd >> 2) + (i16 >> 2);
  const int rot1 = isA ? 0 : 4 * (xcd >> 1) + 8 * (xcd & 1);
  constexpr int WR_OFF = 98304;
  constexpr int PH_PITCH = 136, PH_BYTES = 64 * PH_PITCH;
  constexpr int RED_OFF = 8 * PH_BYTES;
  constexpr int RED_W = 8192 + 256;
  constexpr int RED_GAP = 6912;
  constexpr int KVF_OFF = 147456;
  constexpr int CNT_OFF = 163824;
  constexpr int SC_OFF = 98304;
  constexpr int SB1 = 49152;
  constexpr int TE_OFF = 3 * SB1;
  constexpr int TP_OFF = 131072;
  constexpr int TC_OFF = TE_OFF + 8192;

  if (wave >= 8) {
    const int lw = wave - 8;
    const int r5 = 8 * lw + (lane >> 3);
    const unsigned rowchunk = (unsigned)(((lane & 7) ^ ((r5 >> 1) & 7)) * 16);
    LAS char* dst0 = sm + lw * 1024;
    if (isA) {
      const unsigned voffA = (unsigned)r5 * 2048 + rowchunk;
      const unsigned voffB = (unsigned)r5 * 6144 + (unsigned)(((lane & 7) ^ ((((r5 >> 1) & 1) | (((r5 >> 3) & 1) << 1)) << 1)) * 16);
      const char* gA = (const char*)Ax + (size_t)rt * 256 * 2048;
      const char* gB = (const char*)Wqkv + (size_t)cg * 128;
      auto issX = [&](int kt, int buf) {
        const rsrc_t r = mk_rsrc(gA, kt < 16 ? 256 * 2048 : 0);
#pragma unroll
        for (int i = 0; i < 8; ++i) dma16(r, dst0 + buf + i * 4096, voffA, ((kt + rot1) & 15) * 128 + i * 32 * 2048);
      };
      auto issW = [&](int j, int kt, int u) {
        const rsrc_t r = mk_rsrc(gB, kt < 16 ? 1024 * 6144 - cg * 128 : 0);
#pragma unroll
        for (int i = 0; i < 2; ++i) dma16(r, dst0 + WR_OFF + (u & 7) * 8192 + i * 4096, voffB, (((kt + rot1) & 15) * 64 + i * 32) * 6144 + j * 2048);
      };
      issX(0, 0); issW(0, 0, 0); issW(1, 0, 1); issW(2, 0, 2);
      u64 epoch = __hip_atomic_load(flagp(ws, FL_EPOCH_A), RLX_AGENT);
      u16 wraw[8];
#pragma unroll
      for (int j = 0; j < 8; ++j) wraw[j] = Wphi[(size_t)(2 * cg + (lw >> 1)) * 1024 + (16 * (j >> 2) + 4 * (lane >> 4) + (j & 3)) * 32 + 16 * (lw & 1) + (lane & 15)];
      cfence();
      issX(1, 32768); issW(0, 1, 3); issW(1, 1, 4);
      int xw = 65536, u0 = 0;
#pragma unroll 1
      for (int t = 0; t < 16; ++t) {
        wait_vm<12>(); bar(); cfence();
        issW(2, t + 1, u0 + 5); issX(t + 2, xw); issW(0, t + 2, u0 + 6); issW(1, t + 2, u0 + 7);
        xw += 32768; if (xw == 98304) xw = 0;
        u0 = (u0 + 3) & 7;
      }
      wait_vm<0>();
      {
        s16x8 w8;
#pragma unroll
        for (int j = 0; j < 8; ++j) w8[j] = (short)wraw[j];
        *(LAS s16x8*)(sm + WR_OFF + 2 * 8192 + lw * 1024 + lane * 16) = w8;
      }
      const u64 tag = epoch + 1ull;
      sync_lds();
      __builtin_amdgcn_s_setprio(3);
      bar(); cfence();
      if (tid == 512) *(LAS u32x4*)(sm + CNT_OFF) = (u32x4){0u, 0u, 0u, 0u};
      STAMP(3);
      {
        const rsrc_t kvrs = mk_rsrc(ws + WS_KVP + (size_t)(cg * 8 + rt) * KVP_BYTES, KVP_BYTES);
#pragma unroll
        for (int j = 0; j < 3; ++j) {
          if (j < 2 || lw == 0) {
            const int zl = lane & 15, zh = zl >> 3;
            f32x4 s0 = {0.f, 0.f, 0.f, 0.f}, s1 = {0.f, 0.f, 0.f, 0.f};
#pragma unroll
            for (int w = 0; w < 4; ++w) {
              if (j < 2) {
                LAS char* rp = sm + RED_OFF + (2 * w + j) * RED_W + (2 * w + j >= 5 ? RED_GAP : 0) + (2 * lw) * 1024 + lane * 16;
                s0 += *(LAS f32x4*)rp; s1 += *(LAS f32x4*)(rp + 1024);
              } else {
                LAS char* rp = sm + RED_OFF + (2 * w + zh) * RED_W + (2 * w + zh >= 5 ? RED_GAP : 0) + 8192 + (zl & 7) * 32;
                s0 += *(LAS f32x4*)rp; s1 += *(LAS f32x4*)(rp + 16);
              }
            }
            const u32x4 d = {pk2(s0[0], s0[1]), pk2(s0[2], s0[3]), pk2(s1[0], s1[1]), pk2(s1[2], s1[3])};
            if (j < 2) __builtin_amdgcn_raw_buffer_store_b128(d, kvrs, ((j * 4 + lw) * 64 + lane) * 16, 0, 16);
            else if (lane < 16) __builtin_amdgcn_raw_buffer_store_b128(d, kvrs, 8192 + zl * 16, 0, 16);
          }
        }
        STAMP(4);
        wait_vm<0>();
        STAMP(5);
        if (lane == 0) __hip_atomic_store(flagp(ws, FL_KV + cg * 256 + rt * 32 + lw * 8), tag, RLX_AGENT);
        if (lw == 0) poll_flag(flagp(ws, FL_KV + cg * 256 + (lane & 31) * 8), tag);
      }
      STAMP(6);
      __builtin_amdgcn_s_setprio(0);
      sync_lds();
      bar(); cfence();
      phase2_load<true>(sm, ws, Wo, Wo, rt2, ct2, lw, lane, 4 * (xcd & 3) + 8 * (xcd >> 2), tag, rt2 == 0 && ct2 == 0, g_st);
    } else {
      const int tloc = 16 * ((r5 >> 2) & 3) + 8 * (r5 & 1) + ((r5 >> 1) & 1) + 2 * (r5 >> 4);
      const unsigned voffA = (unsigned)tloc * 2048 + rowchunk;
      const unsigned voffB = (unsigned)r5 * 2048 + rowchunk;
      const char* gA = (const char*)Bx + (size_t)rt * 256 * 2048;
      const char* gBr = (const char*)Bre + (size_t)cg * 64 * 2048;
      const char* gBi = (const char*)Bim + (size_t)cg * 64 * 2048;
      auto issue = [&](int kt, int buf) {
        const bool live = kt < 16;
        kt = (kt + rot1) & 15;
        const rsrc_t ra = mk_rsrc(gA, live ? 256 * 2048 : 0), rr = mk_rsrc(gBr, live ? 64 * 2048 : 0), ri = mk_rsrc(gBi, live ? 64 * 2048 : 0);
#pragma unroll
        for (int i = 0; i < 8; ++i) dma16(ra, dst0 + buf + i * 4096, voffA, kt * 128 + ((i >> 1) * 64 + (i & 1) * 4) * 2048);
#pragma unroll
        for (int i = 8; i < 12; ++i) dma16(i < 10 ? rr : ri, dst0 + buf + i * 4096, voffB, kt * 128 + (i & 1) * 32 * 2048);
      };
      issue(0, 0);
      u64 epoch = __hip_atomic_load(flagp(ws, FL_EPOCH_B), RLX_AGENT);
      cfence();
      issue(1, SB1);
      int wr = 2 * SB1;
#pragma unroll 1
      for (int t = 0; t < 16; ++t) {
        wait_vm<12>(); bar(); cfence();
        issue(t + 2, wr);
        wr += SB1; if (wr == 3 * SB1) wr = 0;
      }
      wait_vm<0>();
      const u64 tag = epoch + 1ull;
      const unsigned tlo = (unsigned)tag, thi = (unsigned)(tag >> 32);
      STAMP(2);
      bar(); cfence();
      if (tid == 512) *(LAS u32x4*)(sm + CNT_OFF) = (u32x4){0u, 0u, 0u, 0u};
      STAMP(3);
      if (lw == 0) {
        __builtin_amdgcn_s_setprio(3);
        const f32x4 kc = *(LAS f32x4*)(sm + TC_OFF + lane * 48 + 32);
        f32x2 E[16];
#pragma unroll
        for (int s = 0; s < 16; ++s) E[s] = *(LAS f32x2*)(sm + TE_OFF + (s * 64 + lane) * 8);
        float sr = 0.f, si = 0.f;
#pragma unroll
        for (int s = 0; s < 16; ++s) {
          const float nr = fmaf(kc[0], sr, fmaf(-kc[1], si, E[s][0])), ni = fmaf(kc[0], si, fmaf(kc[1], sr, E[s][1]));
          E[s] = (f32x2){sr, si}; sr = nr; si = ni;
        }
        const u32x4 mine = {__float_as_uint(sr), tlo, __float_as_uint(si), thi};
        __builtin_amdgcn_raw_buffer_store_b128(mine, mk_rsrc(ws + WS_AGG + (size_t)cg * 8192, 8192), (rt * 64 + lane) * 16, 0, 16);
#pragma unroll
        for (int s = 0; s < 16; ++s) *(LAS f32x2*)(sm + TE_OFF + (s * 64 + lane) * 8) = E[s];
        const rsrc_t agrs = mk_rsrc(ws + WS_AGG + (size_t)cg * 8192, rt * 1024);
        u32x4 gr[7];
#pragma unroll 1
        for (unsigned spins = 0; spins < SPIN_CAP; ++spins) {
          unsigned bad = 0;
          cfence();
#pragma unroll
          for (int k = 0; k < 7; ++k) gr[k] = __builtin_amdgcn_raw_buffer_load_b128(agrs, (k * 64 + lane) * 16, 0, 16);
#pragma unroll
          for (int k = 0; k < 7; ++k) if (k < rt) bad |= (gr[k][1] ^ tlo) | (gr[k][3] ^ thi);
          if (__all(bad == 0)) break;
          __builtin_amdgcn_s_sleep(POLL_SLEEP);
        }
        sr = 0.f; si = 0.f;
#pragma unroll
        for (int k = 0; k < 7; ++k)
          if (k < rt) {
            const float nr = fmaf(kc[2], sr, fmaf(-kc[3], si, __uint_as_float(gr[k][0]))), ni = fmaf(kc[2], si, fmaf(kc[3], sr, __uint_as_float(gr[k][2])));
            sr = nr; si = ni;
          }
        *(LAS f32x2*)(sm + TP_OFF + 8192 + lane * 8) = (f32x2){sr, si};
        __builtin_amdgcn_s_setprio(0);
      } else if (lw == 1) {
        const f32x4 kc = *(LAS f32x4*)(sm + TC_OFF + lane * 48 + 32);
        float pr = 1.f, pi = 0.f;
#pragma unroll
        for (int s = 0; s < 16; ++s) {
          *(LAS f32x2*)(sm + TP_OFF + (s * 64 + lane) * 8) = (f32x2){pr, pi};
          const float nr = pr * kc[0] - pi * kc[1], ni = pr * kc[1] + pi * kc[0]; pr = nr; pi = ni;
        }
      }
      STAMP(4);
      sync_lds();
      phase2_load<false>(sm, ws, Cre, Cim, rt2, ct2, lw, lane, 8 * (xcd & 3) + 16 * (xcd >> 2), tag, rt2 == 15 && ct2 == 0, g_st);
    }
    return;
  }

  const int wm = wave >> 1, wn = wave & 1;
  const int q = (lane & 15) >> 2, p4 = lane & 3;

  if (isA) {
    u64 epoch = __hip_atomic_load(flagp(ws, FL_EPOCH_A), RLX_AGENT);
    const int r16 = lane & 15, g4 = lane >> 4;
    int ro16[2], tofs[2];
#pragma unroll
    for (int s = 0; s < 2; ++s) {
      ro16[s] = wm * 8192 + r16 * 128 + 16 * ((4 * s + g4) ^ ((r16 >> 1) & 7));
      tofs[s] = WR_OFF + (8 * g4 + q) * 128 + 8 * p4 + 32 * ((2 * wn + s) ^ (((q >> 1) & 1) | ((g4 & 1) << 1)));
    }
    f32x4 Qt[2][4], Kt[2][4], V[4][2];
#pragma unroll
    for (int i = 0; i < 4; ++i)
#pragma unroll
      for (int j = 0; j < 2; ++j) { Qt[j][i] = (f32x4){0.f, 0.f, 0.f, 0.f}; Kt[j][i] = (f32x4){0.f, 0.f, 0.f, 0.f}; V[i][j] = (f32x4){0.f, 0.f, 0.f, 0.f}; }
    STAMP(1);
    {
      bf16x8 fx[4];
      s16x4 fw[6][2];
      fx[2] = zero8(); fx[3] = zero8();
#pragma unroll
      for (int j = 0; j < 6; ++j) { fw[j][0] = (s16x4){0, 0, 0, 0}; fw[j][1] = (s16x4){0, 0, 0, 0}; }
      auto mma = [&](auto TT, auto J) {
        constexpr int tt = decltype(TT)::v, j = decltype(J)::v;
        const bf16x8 w = cat8(fw[j][0], fw[j][1]);
        if constexpr (j < 2) Qt[j][tt] = MFMA16(w, fx[tt], Qt[j][tt]);
        else if constexpr (j < 4) Kt[j - 2][tt] = MFMA16(w, fx[tt], Kt[j - 2][tt]);
        else V[tt][j - 4] = MFMA16(fx[tt], w, V[tt][j - 4]);
      };
      int xr = 0, u0 = 0;
#pragma unroll 1
      for (int t = 0; t < 16; ++t) {
        wait_lgkm0(); bar(); cfence(); SCHED();
        LAS char* xb = sm + xr;
        const int wo_[3] = {(u0 & 7) * 8192, ((u0 + 1) & 7) * 8192, ((u0 + 2) & 7) * 8192};
        auto pass1 = [&](auto NS) {
          constexpr int ns = decltype(NS)::v;
          sfor<0, 6>([&](auto J) {
            constexpr int j = decltype(J)::v;
            mma(IC<2>{}, J); SCHED();
            if constexpr (j < 2) { fx[j] = lds_b128(xb + ro16[ns] + j * 2048); SCHED(); }
            mma(IC<3>{}, J); SCHED();
            LAS char* wp = sm + tofs[j & 1] + wo_[j >> 1] + ns * 4096;
            fw[j][0] = lds_tr(wp); fw[j][1] = lds_tr(wp + 512); SCHED();
          });
        };
        auto pass0 = [&](auto S_) {
          constexpr int s = decltype(S_)::v;
          sfor<0, 6>([&](auto J) {
            constexpr int j = decltype(J)::v;
            mma(IC<0>{}, J); SCHED();
            if constexpr (j < 2) { fx[2 + j] = lds_b128(xb + ro16[s] + (2 + j) * 2048); SCHED(); }
            mma(IC<1>{}, J); SCHED();
          });
        };
        pass1(IC<0>{}); pass0(IC<0>{}); pass1(IC<1>{}); pass0(IC<1>{});
        xr += 32768; if (xr == 98304) xr = 0;
        u0 = (u0 + 3) & 7;
      }
      sfor<0, 6>([&](auto J) { mma(IC<2>{}, J); mma(IC<3>{}, J); });
    }
    asm volatile("" : "+v"(epoch));
    const u64 tag = epoch + 1ull;
    STAMP(2);
    sync_lds();
    STAMP(3);
    const bf16x8 wf0 = lds_b128(sm + WR_OFF + 2 * 8192 + (2 * wn) * 1024 + lane * 16);
    const bf16x8 wf1 = lds_b128(sm + WR_OFF + 2 * 8192 + (2 * wn + 1) * 1024 + lane * 16);
    const f32x4 zero4 = {0.f, 0.f, 0.f, 0.f};
    s16x8 o8;
#pragma unroll
    for (int j = 0; j < 8; ++j) o8[j] = (short)0x3f80;
    const bf16x8 ones = __builtin_bit_cast(bf16x8, o8);
    LAS char* ph = sm + wave * PH_BYTES;
#pragma unroll
    for (int tt = 0; tt < 4; ++tt) {
      const bf16x8 kb = pack44(Kt[0][tt], Kt[1][tt]);
      const f32x4 pk[2] = {MFMA16(wf0, kb, zero4), MFMA16(wf1, kb, zero4)};
      f32x4 pp[2], pm[2];
      dual_exp<true>(pk, pp, pm);
      LAS char* rowp = ph + (16 * tt + r16) * PH_PITCH + 8 * g4;
#pragma unroll
      for (int f = 0; f < 2; ++f) {
        *(LAS u32x2*)(rowp + 32 * f) = (u32x2){pk2(pp[f][0], pp[f][1]), pk2(pp[f][2], pp[f][3])};
        *(LAS u32x2*)(rowp + 64 + 32 * f) = (u32x2){pk2(pm[f][0], pm[f][1]), pk2(pm[f][2], pm[f][3])};
      }
    }
    wait_lgkm0();
    STAMP(4);
    {
      f32x4 KV[4][2], Z[4];
#pragma unroll
      for (int fs = 0; fs < 4; ++fs) { KV[fs][0] = zero4; KV[fs][1] = zero4; Z[fs] = zero4; }
      LAS char* tp = ph + (4 * g4 + q) * PH_PITCH + 8 * p4;
#pragma unroll
      for (int u = 0; u < 2; ++u) {
        const bf16x8 vb0 = pack44(V[2 * u][0], V[2 * u + 1][0]), vb1 = pack44(V[2 * u][1], V[2 * u + 1][1]);
#pragma unroll
        for (int fs = 0; fs < 4; ++fs) {
          LAS char* t0 = tp + 32 * u * PH_PITCH + 32 * fs;
          const bf16x8 af = cat8(lds_tr(t0), lds_tr(t0 + 16 * PH_PITCH));
          KV[fs][0] = MFMA16(af, vb0, KV[fs][0]); KV[fs][1] = MFMA16(af, vb1, KV[fs][1]);
          Z[fs] = MFMA16(af, ones, Z[fs]);
        }
      }
      LAS char* rw = sm + RED_OFF + wave * RED_W + (wave >= 5 ? RED_GAP : 0);
#pragma unroll
      for (int fs = 0; fs < 4; ++fs)
#pragma unroll
        for (int dt = 0; dt < 2; ++dt) *(LAS f32x4*)(rw + ((((fs >> 1) * 2 + dt) * 2) + (fs & 1)) * 1024 + lane * 16) = KV[fs][dt];
      if (r16 == 0) {
#pragma unroll
        for (int fs = 0; fs < 4; ++fs) *(LAS f32x4*)(rw + 8192 + (((fs >> 1) * 4 + g4) * 2 + (fs & 1)) * 16) = Z[fs];
      }
    }
    STAMP(5);
    sync_lds();
    STAMP(6);
    asm volatile("" : "+v"(Qt[0][0]), "+v"(Qt[0][1]), "+v"(Qt[0][2]), "+v"(Qt[0][3]), "+v"(Qt[1][0]), "+v"(Qt[1][1]), "+v"(Qt[1][2]), "+v"(Qt[1][3]));
    bf16x8 fq[4][2];
#pragma unroll
    for (int tt = 0; tt < 4; ++tt) {
      const bf16x8 qb = pack44(Qt[0][tt], Qt[1][tt]);
      const f32x4 pq[2] = {MFMA16(wf0, qb, zero4), MFMA16(wf1, qb, zero4)};
      f32x4 pp[2], pm[2];
      dual_exp<false>(pq, pp, pm);
      fq[tt][0] = pack44(pp[0], pp[1]); fq[tt][1] = pack44(pm[0], pm[1]);
    }
    asm volatile("" : "+v"(fq[0][0]), "+v"(fq[0][1]), "+v"(fq[1][0]), "+v"(fq[1][1]), "+v"(fq[2][0]), "+v"(fq[2][1]), "+v"(fq[3][0]), "+v"(fq[3][1]));
    STAMP(10);
    cfence(); bar(); cfence();
    STAMP(12);
    {
      const rsrc_t kvrs = mk_rsrc(ws + WS_KVP + (size_t)cg * 8 * KVP_BYTES, 8 * KVP_BYTES);
      u32x4 v[8], vz[8];
#pragma unroll
      for (int r = 0; r < 8; ++r) v[r] = __builtin_amdgcn_raw_buffer_load_b128(kvrs, tid * 16, r * KVP_BYTES, 16);
      if (wave == 7) {
#pragma unroll
        for (int r = 0; r < 8; ++r) vz[r] = __builtin_amdgcn_raw_buffer_load_b128(kvrs, 8192 + (lane & 15) * 16, r * KVP_BYTES, 16);
      }
      auto sum8 = [&](const u32x4 (&x)[8]) {
        f32x4 a = {0.f, 0.f, 0.f, 0.f}, c = {0.f, 0.f, 0.f, 0.f};
#pragma unroll
        for (int r = 0; r < 8; ++r) {
          a += (f32x4){bflo(x[r][0]), bfhi(x[r][0]), bflo(x[r][1]), bfhi(x[r][1])};
          c += (f32x4){bflo(x[r][2]), bfhi(x[r][2]), bflo(x[r][3]), bfhi(x[r][3])};
        }
        return (u32x4){pk2(a[0], a[1]), pk2(a[2], a[3]), pk2(c[0], c[1]), pk2(c[2], c[3])};
      };
      *(LAS u32x4*)(sm + KVF_OFF + tid * 16) = sum8(v);
      if (wave == 7) { const u32x4 z = sum8(vz); if (lane < 16) *(LAS u32x4*)(sm + KVF_OFF + 8192 + lane * 16) = z; }
    }
    STAMP(13);
    sync_lds();
    STAMP(14);
    {
      bf16x8 kf[4], zf[2];
#pragma unroll
      for (int ks = 0; ks < 4; ++ks) kf[ks] = lds_b128(sm + KVF_OFF + wn * 4096 + ks * 1024 + lane * 16);
#pragma unroll
      for (int sg = 0; sg < 2; ++sg) {
        const bf16x8 z = lds_b128(sm + KVF_OFF + 8192 + wn * 128 + (sg * 4 + g4) * 16);
        zf[sg] = (lane & 1) ? ones : z;
      }
      LAS char* sc = sm + SC_OFF + wave * 5120;
#pragma unroll
      for (int tt = 0; tt < 4; ++tt) {
        const f32x4 dp = MFMA16(zf[0], fq[tt][0], zero4), dm = MFMA16(zf[1], fq[tt][1], zero4);
        const f32x4 np0 = MFMA16(kf[0], fq[tt][0], zero4), np1 = MFMA16(kf[1], fq[tt][0], zero4);
        const f32x4 nm0 = MFMA16(kf[2], fq[tt][1], zero4), nm1 = MFMA16(kf[3], fq[tt][1], zero4);
        const float rp = __builtin_amdgcn_rcpf(dp[1]), rm = __builtin_amdgcn_rcpf(dm[1]);
        const float rinv = 1.0f / (dp[0] * rp + dm[0] * rm + 1e-6f);
        const float cp = rp * rinv, cm = rm * rinv;
        *(LAS bf16x8*)(sc + (16 * tt + r16) * 80 + g4 * 16) = pack44(np0 * cp + nm0 * cm, np1 * cp + nm1 * cm);
      }
      wait_lgkm0();
      const rsrc_t ors = mk_rsrc(ws + WS_O, 2048 * 2048);
      const int obase = ((2 * cg + wn) * 2048 + rt * 256 + 64 * wm) * 64 + lane * 16;
#pragma unroll
      for (int j = 0; j < 4; ++j) {
        const u32x4 v = *(LAS u32x4*)(sc + (16 * j + (lane >> 2)) * 80 + (lane & 3) * 16);
        __builtin_amdgcn_raw_buffer_store_b128(v, ors, obase + j * 1024, 0, 16);
      }
    }
    STAMP(15);
    wait_vm<0>();
    STAMP(16);
    publish_half((LAS unsigned*)(sm + CNT_OFF) + (wave >> 2), flagp(ws, FL_O + (2 * rt + (wave >> 2)) * 128 + cg * 8), tag, lane);
    phase2<true>(sm, ws, Bx, Dskip, out, rt2, ct2, wave, lane, g_st);
  } else {
    u64 epoch = __hip_atomic_load(flagp(ws, FL_EPOCH_B), RLX_AGENT);
    const uintptr_t pw0 = (uintptr_t)Wqkv, pw1 = (uintptr_t)Cre, pw2 = (uintptr_t)Cim, pw3 = (uintptr_t)Wo;
    if (wave == 7) {
      const int ch = 64 * cg + lane;
      const float lamr = bf2f(Lre[ch]), lami = bf2f(Lim[ch]);
      const float dt = expf(bf2f(Logdt[ch]));
      const float zr = dt * lamr, zi = dt * lami;
      float sn, cs;
      sincosf(zi, &sn, &cs);
      const float mag = expf(zr);
      const float lr = mag * cs, li = mag * sn;
      const float nr = lr - 1.0f, ni = li, dd = 2.0f / (lamr * lamr + lami * lami);
      const float c2r = (nr * lamr + ni * lami) * dd, c2i = (ni * lamr - nr * lami) * dd;
      float pr = lr, pi = li;
#pragma unroll
      for (int i = 0; i < 3; ++i) { const float t = pr * pr - pi * pi; pi = 2.0f * pr * pi; pr = t; }
      const float l8r = pr, l8i = pi;
      { const float t = pr * pr - pi * pi; pi = 2.0f * pr * pi; pr = t; }
      const float l16r = pr, l16i = pi;
#pragma unroll
      for (int i = 0; i < 4; ++i) { const float t = pr * pr - pi * pi; pi = 2.0f * pr * pi; pr = t; }
      LAS f32x4* tc = (LAS f32x4*)(sm + TC_OFF + lane * 48);
      tc[0] = (f32x4){lr, li, c2r, c2i}; tc[1] = (f32x4){l8r, l8i, 0.f, 0.f}; tc[2] = (f32x4){l16r, l16i, pr, pi};
    }
    const int r16 = lane & 15, g4 = lane >> 4;
    int ro16[2];
#pragma unroll
    for (int s = 0; s < 2; ++s) ro16[s] = r16 * 128 + 16 * ((4 * s + g4) ^ ((r16 >> 1) & 7));
    f32x4 U[4][4];
#pragma unroll
    for (int i = 0; i < 4; ++i)
#pragma unroll
      for (int j = 0; j < 4; ++j) U[i][j] = (f32x4){0.f, 0.f, 0.f, 0.f};
    STAMP(1);
    {
      bf16x8 fx[2][4], fb[2][4];
#pragma unroll
      for (int i = 0; i < 4; ++i) { fx[1][i] = zero8(); fb[1][i] = zero8(); }
      auto mma = [&](auto B_, auto I) {
        constexpr int B = decltype(B_)::v, i = decltype(I)::v;
        U[i >> 2][i & 3] = MFMA16(fx[B][i >> 2], fb[B][i & 3], U[i >> 2][i & 3]);
      };
      int rd = 0;
#pragma unroll 1
      for (int t = 0; t < 16; ++t) {
        wait_lgkm0(); bar(); cfence(); SCHED();
        if ((t & 7) == wave && t < 12) {
          const uintptr_t wb_ = t < 6 ? pw0 : (t < 8 ? pw1 : (t < 10 ? pw2 : pw3));
          l2touch(mk_rsrc((const void*)wb_, 0x7fffffff), (unsigned)lane * 128, ((t < 6 ? t : (t & 1)) * 128 + xcd * 16 + i16) * 8192);
        }
        LAS char* xb = sm + rd + wm * 8192;
        LAS char* bb = sm + rd + 32768 + wn * 4096;
        auto rdf = [&](auto B_, auto S_, auto X) {
          constexpr int B = decltype(B_)::v, s = decltype(S_)::v, x = decltype(X)::v;
          if constexpr (x == 0) fx[B][0] = lds_b128(xb + ro16[s]);
          else if constexpr (x < 5) fb[B][x - 1] = lds_b128(bb + ro16[s] + ((x - 1) >> 1) * 8192 + ((x - 1) & 1) * 2048);
          else fx[B][x - 4] = lds_b128(xb + ro16[s] + (x - 4) * 2048);
        };
        mgroup<16, 8, 1>([&](auto i) { mma(IC<1>{}, i); }, [&](auto x) { rdf(IC<0>{}, IC<0>{}, x); });
        mgroup<16, 8, 1>([&](auto i) { mma(IC<0>{}, i); }, [&](auto x) { rdf(IC<1>{}, IC<1>{}, x); });
        rd += SB1; if (rd == 3 * SB1) rd = 0;
      }
      sfor<0, 16>([&](auto i) { mma(IC<1>{}, i); });
    }
    asm volatile("" : "+v"(epoch));
    const u64 tag = epoch + 1ull;
    STAMP(2);
    const int seg = 4 * wm + g4, chl = 32 * wn + r16;
    f32x4 k0[2]; f32x2 k8[2];
#pragma unroll
    for (int n = 0; n < 2; ++n) { k0[n] = *(LAS f32x4*)(sm + TC_OFF + (chl + 16 * n) * 48); k8[n] = *(LAS f32x2*)(sm + TC_OFF + (chl + 16 * n) * 48 + 16); }
    f32x2 e0[2];
#pragma unroll
    for (int n = 0; n < 2; ++n) {
      const f32x2 lr2 = {k0[n][0], k0[n][0]}, li2 = {k0[n][1], k0[n][1]};
      f32x2 er = {0.f, 0.f}, ei = {0.f, 0.f};
#pragma unroll
      for (int sg = 0; sg < 8; ++sg) {
        const f32x2 ur = {U[sg >> 1][n][2 * (sg & 1)], U[sg >> 1][n][2 * (sg & 1) + 1]}, ui = {U[sg >> 1][2 + n][2 * (sg & 1)], U[sg >> 1][2 + n][2 * (sg & 1) + 1]};
        const f32x2 nr = __builtin_elementwise_fma(lr2, er, __builtin_elementwise_fma(-li2, ei, ur));
        const f32x2 ni = __builtin_elementwise_fma(lr2, ei, __builtin_elementwise_fma(li2, er, ui));
        er = nr; ei = ni;
        U[sg >> 1][n][2 * (sg & 1)] = er[0]; U[sg >> 1][n][2 * (sg & 1) + 1] = er[1];
        U[sg >> 1][2 + n][2 * (sg & 1)] = ei[0]; U[sg >> 1][2 + n][2 * (sg & 1) + 1] = ei[1];
      }
      e0[n] = (f32x2){er[0], ei[0]};
      *(LAS f32x2*)(sm + TE_OFF + (seg * 64 + chl + 16 * n) * 8) =
          (f32x2){fmaf(k8[n][0], er[0], fmaf(-k8[n][1], ei[0], er[1])), fmaf(k8[n][0], ei[0], fmaf(k8[n][1], er[0], ei[1]))};
    }
    STAMP(3);
    sync_lds();
    STAMP(4);
#pragma unroll
    for (int i = 0; i < 4; ++i) asm volatile("" : "+v"(U[i][0]), "+v"(U[i][1]), "+v"(U[i][2]), "+v"(U[i][3]));
    asm volatile("" : "+v"(k0[0]), "+v"(k0[1]));
    float Pr[2][8], Pi[2][8];
#pragma unroll
    for (int n = 0; n < 2; ++n) {
      const f32x2 cr2 = {k0[n][2], k0[n][2]}, ci2 = {k0[n][3], k0[n][3]};
      float pr = k0[n][2], pi = k0[n][3];
#pragma unroll
      for (int sg = 0; sg < 8; ++sg) {
        const f32x2 er = {U[sg >> 1][n][2 * (sg & 1)], U[sg >> 1][n][2 * (sg & 1) + 1]}, ei = {U[sg >> 1][2 + n][2 * (sg & 1)], U[sg >> 1][2 + n][2 * (sg & 1) + 1]};
        const f32x2 hr = __builtin_elementwise_fma(cr2, er, -(ci2 * ei)), hi = -__builtin_elementwise_fma(cr2, ei, ci2 * er);
        U[sg >> 1][n][2 * (sg & 1)] = hr[0]; U[sg >> 1][n][2 * (sg & 1) + 1] = hr[1];
        U[sg >> 1][2 + n][2 * (sg & 1)] = hi[0]; U[sg >> 1][2 + n][2 * (sg & 1) + 1] = hi[1];
        const float nr = pr * k0[n][0] - pi * k0[n][1], ni = pr * k0[n][1] + pi * k0[n][0];
        pr = nr; pi = ni; Pr[n][sg] = pr; Pi[n][sg] = pi;
      }
    }
#pragma unroll
    for (int i = 0; i < 4; ++i) asm volatile("" : "+v"(U[i][0]), "+v"(U[i][1]), "+v"(U[i][2]), "+v"(U[i][3]));
    asm volatile("" : "+v"(Pr[0][7]), "+v"(Pr[1][7]), "+v"(Pi[0][7]), "+v"(Pi[1][7]));
    sync_lds();
    STAMP(6);
    {
      const rsrc_t hrs = mk_rsrc(ws + WS_H, 2048 * 4096);
      LAS char* sre = sm + SC_OFF + wave * 4096;
      LAS char* sim = sm + 32768 + (wave >> 2) * 32768 + (wave & 3) * 4096;
#pragma unroll
      for (int n = 0; n < 2; ++n) {
        const f32x2 xl = *(LAS f32x2*)(sm + TE_OFF + (seg * 64 + chl + 16 * n) * 8), pw = *(LAS f32x2*)(sm + TP_OFF + (seg * 64 + chl + 16 * n) * 8);
        const f32x2 hin = *(LAS f32x2*)(sm + TP_OFF + 8192 + (chl + 16 * n) * 8);
        const f32x2 s0 = {fmaf(pw[0], hin[0], fmaf(-pw[1], hin[1], xl[0])), fmaf(pw[0], hin[1], fmaf(pw[1], hin[0], xl[1]))};
        const f32x2 sr = {s0[0], fmaf(k8[n][0], s0[0], fmaf(-k8[n][1], s0[1], e0[n][0]))};
        const f32x2 si = {s0[1], fmaf(k8[n][0], s0[1], fmaf(k8[n][1], s0[0], e0[n][1]))};
        f32x2 hr[8], hi[8];
#pragma unroll
        for (int sg = 0; sg < 8; ++sg) {
          const f32x2 ar = {U[sg >> 1][n][2 * (sg & 1)], U[sg >> 1][n][2 * (sg & 1) + 1]}, ai = {U[sg >> 1][2 + n][2 * (sg & 1)], U[sg >> 1][2 + n][2 * (sg & 1) + 1]};
          const f32x2 pr2 = {Pr[n][sg], Pr[n][sg]}, pi2 = {Pi[n][sg], Pi[n][sg]};
          hr[sg] = __builtin_elementwise_fma(pr2, sr, __builtin_elementwise_fma(-pi2, si, ar));
          hi[sg] = __builtin_elementwise_fma(-pr2, si, __builtin_elementwise_fma(-pi2, sr, ai));
        }
#pragma unroll
        for (int c = 0; c < 2; ++c) {
          const int off = (16 * n + r16) * 128 + (((2 * g4 + c) ^ (lane & 7)) * 16);
          *(LAS u32x4*)(sre + off) = (u32x4){pk2(hr[0][c], hr[1][c]), pk2(hr[2][c], hr[3][c]), pk2(hr[4][c], hr[5][c]), pk2(hr[6][c], hr[7][c])};
          *(LAS u32x4*)(sim + off) = (u32x4){pk2(hi[0][c], hi[1][c]), pk2(hi[2][c], hi[3][c]), pk2(hi[4][c], hi[5][c]), pk2(hi[6][c], hi[7][c])};
        }
      }
      STAMP(5);
      wait_lgkm0();
      const int ro = (lane >> 3) * 128 + (((lane & 7) ^ (lane >> 3)) * 16);
      const int hbase = (cg * 64 + 32 * wn + (lane >> 3)) * 4096 + rt * 512 + wm * 128 + (lane & 7) * 16;
      u32x4 v[8];
#pragma unroll
      for (int j = 0; j < 4; ++j) { v[j] = *(LAS u32x4*)(sre + ro + j * 1024); v[4 + j] = *(LAS u32x4*)(sim + ro + j * 1024); }
#pragma unroll
      for (int j = 0; j < 8; ++j) __builtin_amdgcn_raw_buffer_store_b128(v[j], hrs, hbase + ((j >> 2) * 1024 + 8 * (j & 3)) * 4096, 0, 16);
    }
    STAMP(15);
    wait_vm<0>();
    STAMP(16);
    publish_half((LAS unsigned*)(sm + CNT_OFF) + (wave >> 2), flagp(ws, FL_H + (2 * rt + (wave >> 2)) * 128 + cg * 8), tag, lane);
    phase2<false>(sm, ws, Bx, Dskip, out, rt2, ct2, wave, lane, g_st);
  }
}

extern "C" void kernel_launch(void* const* d_in, const int* in_sizes, int n_in, void* d_out, int out_size, void* d_ws,
                              size_t ws_size, hipStream_t stream) {
  fused_kernel<<<256, 768, 0, stream>>>((const u16*)d_in[0], (const u16*)d_in[1], (const u16*)d_in[2], (const u16*)d_in[3],
                                        (const u16*)d_in[4], (const u16*)d_in[5], (const u16*)d_in[6], (const u16*)d_in[7],
                                        (const u16*)d_in[8], (const u16*)d_in[9], (const u16*)d_in[10], (const u16*)d_in[11],
                                        (const u16*)d_in[12], (float*)d_out, (char*)d_ws);
}
```
